```python
import torch

def _softmax(x, axis=-1):
    return torch.softmax(x, dim=axis)
def _gelu(x):
    return torch.nn.functional.gelu(x, approximate="tanh")
def _silu(x):
    return torch.nn.functional.silu(x)
def _sigmoid(x):
    return torch.sigmoid(x)
def _layernorm(x, axis=-1, eps=1e-5):
    m = x.mean(dim=axis, keepdim=True)
    v = x.var(dim=axis, keepdim=True, unbiased=False)
    return (x - m) / torch.sqrt(v + eps)
def _scatter_add(tgt, idx, src, axis=0):
    return tgt.clone().index_add_(axis, idx, src)
def _segment_sum(src, seg, n):
    out = torch.zeros((n,) + src.shape[1:], dtype=src.dtype)
    return out.index_add_(0, seg, src)
def _pad(x, pw):
    flat = tuple(v for pair in reversed(pw) for v in pair)
    return torch.nn.functional.pad(x, flat)

kernel_name = 'standalone__mha_block_cached__near_ridge'

def setup_inputs():

    _w = (lambda a: (lambda t: t.to(torch.bfloat16) if t.is_floating_point() else t)(torch.as_tensor(a)))
    B, Sq, D = 8, 128, 1024
    H, Sk, Dh = 32, 2048, 128
    return {
        "x": _w((lambda *s: torch.randn(*s, dtype=torch.bfloat16) * 0.8)(B, Sq, D)),
        "k_cache": _w((lambda *s: torch.randn(*s, dtype=torch.bfloat16) * 0.8)(B, H, Sk, Dh)),
        "v_cache": _w((lambda *s: torch.randn(*s, dtype=torch.bfloat16) * 0.05)(B, H, Sk, Dh)),
        "wq": _w((lambda *s: torch.randn(*s, dtype=torch.bfloat16) * 0.8)(D, H * Dh) * (D ** -0.5)),
        "wo": _w((lambda *s: torch.randn(*s, dtype=torch.bfloat16) * 0.05)(H * Dh, D) * ((H * Dh) ** -0.5)),
    }

def reference(x, k_cache, v_cache, wq, wo):
    B, Sq, D = x.shape
    H, Dh = 32, 128
    q = (lambda x, *d: x.permute(*d))((x @ wq).reshape(B, Sq, H, Dh), 0, 2, 1, 3)
    scores = torch.einsum("bhqd,bhkd->bhqk", q, k_cache) * (Dh ** -0.5)
    p = _softmax(scores, axis=-1)
    o = torch.einsum("bhqk,bhkd->bhqd", p, v_cache)
    return (lambda x, *d: x.permute(*d))(o, 0, 2, 1, 3).reshape(B, Sq, H * Dh) @ wo
```

```cpp
#include <hip/hip_runtime.h>
#include <hip/hip_bf16.h>
#include <stdint.h>

typedef float    f32x16 __attribute__((ext_vector_type(16)));
typedef float    f32x4  __attribute__((ext_vector_type(4)));
typedef float    f32x2  __attribute__((ext_vector_type(2)));
typedef __bf16   bf16x8 __attribute__((ext_vector_type(8)));
typedef __bf16   bf16x2 __attribute__((ext_vector_type(2)));
typedef short    s16x4  __attribute__((ext_vector_type(4)));
typedef short    s16x8  __attribute__((ext_vector_type(8)));
typedef unsigned u32x4  __attribute__((ext_vector_type(4)));
typedef unsigned u32x2  __attribute__((ext_vector_type(2)));
typedef __attribute__((address_space(3))) char lds_char;

#define DI __device__ __forceinline__
#define SBAR() __builtin_amdgcn_sched_barrier(0)

constexpr int B_ = 8, SQ = 128, DM = 1024, NH = 32, SK = 2048, DH = 128;
constexpr int KVT = 64;
constexpr int NTILE = SK / KVT;
constexpr int NSLOT = 4;
constexpr int NPRE = 3;
constexpr unsigned WRING = 98304;
constexpr unsigned QIMG = 131072;
constexpr int LDS1 = 163840;
constexpr float QSCALE = 0.08838834764831845f * 1.4426950408889634f;

DI unsigned rfl(unsigned x) { return __builtin_amdgcn_readfirstlane(x); }
DI unsigned cvtpk(float lo, float hi) { f32x2 v = {lo, hi}; bf16x2 b = __builtin_convertvector(v, bf16x2); return __builtin_bit_cast(unsigned, b); }

template <bool NT> DI void dma16(unsigned voff, const void* sbase, unsigned ldsdst) {
  if constexpr (NT) asm volatile("s_mov_b32 m0, %2\n\ts_nop 0\n\tglobal_load_lds_dwordx4 %0, %1 nt" :: "v"(voff), "s"(sbase), "s"(ldsdst) : "memory");
  else              asm volatile("s_mov_b32 m0, %2\n\ts_nop 0\n\tglobal_load_lds_dwordx4 %0, %1" :: "v"(voff), "s"(sbase), "s"(ldsdst) : "memory");
}
DI bf16x8 lds_rd128(const lds_char* p) { return *(const __attribute__((address_space(3))) bf16x8*)p; }
DI s16x4 lds_tr(const lds_char* p) { return __builtin_amdgcn_ds_read_tr16_b64_v4i16((__attribute__((address_space(3))) s16x4*)p); }
DI bf16x8 cat8(s16x4 a, s16x4 b) { s16x8 r = __builtin_shufflevector(a, b, 0, 1, 2, 3, 4, 5, 6, 7); return __builtin_bit_cast(bf16x8, r); }
#define MFMA32(a, b, c) __builtin_amdgcn_mfma_f32_32x32x16_bf16(a, b, c, 0, 0, 0)
#define MFMA16(a, b, c) __builtin_amdgcn_mfma_f32_16x16x32_bf16(a, b, c, 0, 0, 0)
#define WAIT_BAR(N) asm volatile("s_waitcnt vmcnt(" #N ") lgkmcnt(0)\n\ts_barrier" ::: "memory")
#define BAR_ONLY() asm volatile("s_waitcnt lgkmcnt(0)\n\ts_barrier" ::: "memory")

template <int I> DI void xload(bf16x8& dst, unsigned voff, const void* sbase) {
  asm volatile("global_load_dwordx4 %0, %1, %2 offset:%3" : "=v"(dst) : "v"(voff), "s"(sbase), "i"(I * 64) : "memory");
}

__global__ __launch_bounds__(512, 2)
void attn_kernel(const char* __restrict__ x, const char* __restrict__ kc, const char* __restrict__ vc,
                 const char* __restrict__ wq, char* __restrict__ ows) {
  extern __shared__ __attribute__((aligned(16))) char smem[];
  lds_char* lds = (lds_char*)smem;
  const unsigned lds0 = (unsigned)(uintptr_t)lds;
  const int tid = threadIdx.x, lane = tid & 63;
  const int wid = rfl(tid >> 6);
  const int bid = blockIdx.x;
  const int h = (bid & 7) * 4 + ((bid >> 3) & 3), b = bid >> 5;
  const int bh = b * NH + h;

  bf16x8 xf[32];
  {
    const char* xb = x + ((size_t)b * SQ + wid * 16) * (DM * 2);
    const unsigned xo = (lane & 15) * (DM * 2) + (lane >> 4) * 16;
#define XL(i) xload<i>(xf[i], xo, xb)
    XL(0); XL(1); XL(2); XL(3); XL(4); XL(5); XL(6); XL(7); XL(8); XL(9); XL(10); XL(11); XL(12); XL(13); XL(14); XL(15);
    XL(16); XL(17); XL(18); XL(19); XL(20); XL(21); XL(22); XL(23); XL(24); XL(25); XL(26); XL(27); XL(28); XL(29); XL(30); XL(31);
#undef XL
  }
  const char* kbase = kc + (size_t)bh * (SK * DH * 2);
  const char* vbase = vc + (size_t)bh * (SK * DH * 2);
  const unsigned rr = lane >> 4, p16 = lane & 15;
  const unsigned ksrc0 = (8 * wid + rr) * 256 + ((p16 ^ ((8 * wid + rr) & 15)) * 16);
  const unsigned ksrc1 = (8 * wid + 4 + rr) * 256 + ((p16 ^ ((8 * wid + 4 + rr) & 15)) * 16);
  const unsigned vsrc0 = (8 * wid + rr) * 256 + ((p16 ^ (rr << 2)) * 16);
  const char* wbase = wq + h * 256;
  const unsigned wsrcA = (16 * (wid & 3) + rr) * 8192 + ((p16 ^ (rr << 1)) * 16);
  const unsigned wsrcB = (16 * (wid & 3) + 8 + rr) * 8192 + ((p16 ^ ((rr | 4) << 1)) * 16);
  auto issue_w = [&](int i) {
    const char* sb = wbase + (size_t)i * (64 * 8192);
    const unsigned dst = lds0 + WRING + (i & 3) * 16384 + (wid & 3) * 4096;
    dma16<false>(wsrcA, sb, dst);
    dma16<false>(wsrcA, sb + 4 * 8192, dst + 1024);
    dma16<false>(wsrcB, sb, dst + 2048);
    dma16<false>(wsrcB, sb + 4 * 8192, dst + 3072);
  };
  auto issue_kv = [&](int t) {
    const unsigned dst = lds0 + (t % NSLOT) * 32768 + wid * 2048;
    const char* kb = kbase + t * 16384; const char* vb = vbase + t * 16384;
    dma16<true>(ksrc0, kb, dst);
    dma16<true>(ksrc1, kb, dst + 1024);
    dma16<true>(vsrc0, vb, dst + 16384);
    dma16<true>(vsrc0, vb + 1024, dst + 16384 + 1024);
  };
  if (wid < 4) {
    issue_w(0); issue_w(1); issue_w(2);
  } else {
#pragma unroll
    for (int t = 0; t < NPRE; ++t) {
      const unsigned dst = lds0 + t * 32768 + wid * 2048;
      const char* kb = kbase + t * 16384; const char* vb = vbase + t * 16384;
      dma16<true>(ksrc0, kb, dst);
      dma16<true>(ksrc1, kb, dst + 1024);
      dma16<true>(ksrc0, kb - 8192, dst - 8192);
      dma16<true>(ksrc1, kb - 8192, dst - 8192 + 1024);
      dma16<true>(vsrc0, vb, dst + 16384);
      dma16<true>(vsrc0, vb + 1024, dst + 16384 + 1024);
      dma16<true>(vsrc0, vb - 8192, dst + 16384 - 8192);
      dma16<true>(vsrc0, vb - 8192 + 1024, dst + 16384 - 8192 + 1024);
    }
  }

  f32x4 qacc[8];
#pragma unroll
  for (int i = 0; i < 8; ++i) qacc[i] = (f32x4){0.f, 0.f, 0.f, 0.f};
  {
    const unsigned g = lane >> 4, q4 = (lane & 15) >> 2, p = lane & 3;
    const unsigned wrd = WRING + (8 * g + q4) * 256 + ((q4 | ((g & 1) << 2)) * 32) + 8 * p;
    if (wid < 4) asm volatile("s_waitcnt vmcnt(12)" ::: "memory"); else asm volatile("s_waitcnt vmcnt(24)" ::: "memory");
#define XP(i) "+v"(xf[i])
    asm volatile("" : XP(0), XP(1), XP(2), XP(3), XP(4), XP(5), XP(6), XP(7), XP(8), XP(9), XP(10), XP(11), XP(12), XP(13), XP(14), XP(15));
    asm volatile("" : XP(16), XP(17), XP(18), XP(19), XP(20), XP(21), XP(22), XP(23), XP(24), XP(25), XP(26), XP(27), XP(28), XP(29), XP(30), XP(31));
#undef XP
#pragma unroll
    for (int i = 0; i < 16; ++i) {
      if (wid < 4) {
        if (i < 14) asm volatile("s_waitcnt vmcnt(8)" ::: "memory");
        else if (i == 14) asm volatile("s_waitcnt vmcnt(4)" ::: "memory");
        else asm volatile("s_waitcnt vmcnt(0)" ::: "memory");
      }
      BAR_ONLY();
      if (i + 3 < 16 && wid < 4) issue_w(i + 3);
#pragma unroll
      for (int ks = 0; ks < 2; ++ks) {
#pragma unroll
        for (int db = 0; db < 8; ++db) {
          const lds_char* a = lds + ((wrd ^ (db << 5)) + (i & 3) * 16384 + ks * 8192);
          bf16x8 af = cat8(lds_tr(a), lds_tr(a + 1024));
          qacc[db] = MFMA16(af, xf[2 * i + ks], qacc[db]);
        }
      }
    }
    BAR_ONLY();
    const unsigned row = 16 * wid + (lane & 15);
#pragma unroll
    for (int db = 0; db < 8; ++db) {
      u32x2 w = {cvtpk(qacc[db][0] * QSCALE, qacc[db][1] * QSCALE), cvtpk(qacc[db][2] * QSCALE, qacc[db][3] * QSCALE)};
      const unsigned ch = 2 * db + (g >> 1);
      *(__attribute__((address_space(3))) u32x2*)(lds + QIMG + row * 256 + ((ch ^ (row & 15)) * 16) + 8 * (g & 1)) = w;
    }
    BAR_ONLY();
  }
  const int qb = wid & 3, kh = wid >> 2;
  const unsigned r32 = lane & 31, hh = lane >> 5;
  bf16x8 qf[8];
  {
    const unsigned row = 32 * qb + r32;
#pragma unroll
    for (int s = 0; s < 8; ++s) qf[s] = lds_rd128(lds + QIMG + row * 256 + (((2 * s + hh) ^ (row & 15)) * 16));
  }

  f32x16 o[4];
#pragma unroll
  for (int i = 0; i < 4; ++i)
#pragma unroll
    for (int r = 0; r < 16; ++r) o[i][r] = 0.f;
  float mrun = -__builtin_inff(), lsum = 0.f;
  const unsigned krd = (32 * kh + r32) * 256 + ((hh ^ (r32 & 15)) * 16);
  const unsigned vq = (lane & 15) >> 2;
  const unsigned vrd = 16384 + (32 * kh + 4 * hh + vq) * 256 + 64 * vq + 32 * ((lane >> 4) & 1) + 8 * (lane & 3);

  for (int t = 0; t < NTILE; ++t) {
    if (t == 0) { if (wid >= 4) asm volatile("s_waitcnt vmcnt(16)" ::: "memory"); BAR_ONLY(); }
    else if (t == 1) { if (wid >= 4) asm volatile("s_waitcnt vmcnt(12)" ::: "memory"); BAR_ONLY(); }
    else if (t + 2 < NTILE) WAIT_BAR(8);
    else if (t + 2 == NTILE) WAIT_BAR(4);
    else WAIT_BAR(0);
    if (t + NPRE < NTILE) issue_kv(t + NPRE);
    const unsigned slot = (t % NSLOT) * 32768;
    f32x16 S;
#pragma unroll
    for (int r = 0; r < 16; ++r) S[r] = 0.f;
#pragma unroll
    for (int s = 0; s < 8; ++s) {
      bf16x8 kf = lds_rd128(lds + slot + (krd ^ (32 * s)));
      S = MFMA32(kf, qf[s], S);
    }
    float mx = fmaxf(fmaxf(S[0], S[1]), S[2]);
#pragma unroll
    for (int r = 3; r < 15; r += 2) mx = fmaxf(fmaxf(mx, S[r]), S[r + 1]);
    mx = fmaxf(mx, S[15]);
    { auto sw = __builtin_amdgcn_permlane32_swap(__float_as_uint(mx), __float_as_uint(mx), false, false);
      mx = fmaxf(__uint_as_float(sw[0]), __uint_as_float(sw[1])); }
    if (__any(mx > mrun)) {
      const float mn = fmaxf(mrun, mx);
      const float al = __builtin_amdgcn_exp2f(mrun - mn);
      mrun = mn; lsum *= al;
#pragma unroll
      for (int i = 0; i < 4; ++i)
#pragma unroll
        for (int r = 0; r < 16; ++r) o[i][r] *= al;
    }
    float ps = 0.f;
#pragma unroll
    for (int r = 0; r < 16; ++r) { S[r] = __builtin_amdgcn_exp2f(S[r] - mrun); ps += S[r]; }
    lsum += ps;
    bf16x8 pf[2];
#pragma unroll
    for (int s = 0; s < 2; ++s) {
      u32x4 w = {cvtpk(S[8 * s], S[8 * s + 1]), cvtpk(S[8 * s + 2], S[8 * s + 3]), cvtpk(S[8 * s + 4], S[8 * s + 5]), cvtpk(S[8 * s + 6], S[8 * s + 7])};
      pf[s] = __builtin_bit_cast(bf16x8, w);
    }
#pragma unroll
    for (int s = 0; s < 2; ++s)
#pragma unroll
      for (int db = 0; db < 4; ++db) {
        const lds_char* a = lds + slot + ((vrd ^ (64 * db)) + 4096 * s);
        bf16x8 vf = cat8(lds_tr(a), lds_tr(a + 2048));
        o[db] = MFMA32(vf, pf[s], o[db]);
      }
  }

  BAR_ONLY();
  {
    { auto sw = __builtin_amdgcn_permlane32_swap(__float_as_uint(lsum), __float_as_uint(lsum), false, false);
      lsum = __uint_as_float(sw[0]) + __uint_as_float(sw[1]); }
    __attribute__((address_space(3))) float* ex = (__attribute__((address_space(3))) float*)(lds + wid * 8192);
    __attribute__((address_space(3))) float* st = (__attribute__((address_space(3))) float*)(lds + 65536 + wid * 512);
#pragma unroll
    for (int j = 0; j < 2; ++j)
#pragma unroll
      for (int r4 = 0; r4 < 4; ++r4) {
        f32x4 v;
        if (kh == 0) v = (f32x4){o[2 + j][4 * r4], o[2 + j][4 * r4 + 1], o[2 + j][4 * r4 + 2], o[2 + j][4 * r4 + 3]};
        else         v = (f32x4){o[j][4 * r4], o[j][4 * r4 + 1], o[j][4 * r4 + 2], o[j][4 * r4 + 3]};
        *(__attribute__((address_space(3))) f32x4*)(ex + (j * 4 + r4) * 256 + lane * 4) = v;
      }
    st[lane] = mrun; st[64 + lane] = lsum;
    BAR_ONLY();
    const int pw = wid ^ 4;
    const __attribute__((address_space(3))) float* pex = (const __attribute__((address_space(3))) float*)(lds + pw * 8192);
    const __attribute__((address_space(3))) float* pst = (const __attribute__((address_space(3))) float*)(lds + 65536 + pw * 512);
    const float m2 = pst[lane], l2 = pst[64 + lane];
    const float mn = fmaxf(mrun, m2);
    const float a1 = __builtin_amdgcn_exp2f(mrun - mn), a2 = __builtin_amdgcn_exp2f(m2 - mn);
    const float inv = 1.0f / (lsum * a1 + l2 * a2);
    const float f1 = a1 * inv, f2 = a2 * inv;
    char* orow = ows + ((size_t)(b * SQ + 32 * qb + r32) * (NH * DH) + h * DH + 64 * kh + 4 * hh) * 2;
#pragma unroll
    for (int j = 0; j < 2; ++j)
#pragma unroll
      for (int r4 = 0; r4 < 4; ++r4) {
        const f32x4 pv = *(const __attribute__((address_space(3))) f32x4*)(pex + (j * 4 + r4) * 256 + lane * 4);
        float mine[4];
#pragma unroll
        for (int e = 0; e < 4; ++e) mine[e] = kh == 0 ? o[j][4 * r4 + e] : o[2 + j][4 * r4 + e];
        u32x2 w = {cvtpk(mine[0] * f1 + pv[0] * f2, mine[1] * f1 + pv[1] * f2), cvtpk(mine[2] * f1 + pv[2] * f2, mine[3] * f1 + pv[3] * f2)};
        *(u32x2*)(orow + (32 * j + 8 * r4) * 2) = w;
      }
  }
}

constexpr int PS = 4;
__global__ __launch_bounds__(512, 2)
void oproj_kernel(const char* __restrict__ ows, const char* __restrict__ wo, char* __restrict__ out) {
  extern __shared__ __attribute__((aligned(16))) char smem[];
  lds_char* lds = (lds_char*)smem;
  const unsigned lds0 = (unsigned)(uintptr_t)lds;
  const int tid = threadIdx.x, lane = tid & 63;
  const int wid = rfl(tid >> 6);
  const int bid = blockIdx.x;
  const int xcd = bid & 7, j = bid >> 3;
  const int tm = (xcd & 1) * 8 + (j & 7), tn = (xcd >> 1) * 4 + (j >> 3);
  const unsigned rr = lane >> 4, p16 = lane & 15;
  const char* abase = ows + (size_t)tm * 64 * 8192;
  const unsigned asrc0 = (8 * wid + rr) * 8192 + ((p16 ^ ((8 * wid + rr) & 15)) * 16);
  const unsigned asrc1 = (8 * wid + 4 + rr) * 8192 + ((p16 ^ ((8 * wid + 4 + rr) & 15)) * 16);
  const char* bbase = wo + tn * 128;
  const unsigned br = lane >> 3, bp = lane & 7;
  const unsigned bsrc = (16 * wid + br) * 2048 + ((bp ^ (((br >> 1) & 1) << 2)) * 16);
  auto issue = [&](int t) {
    const unsigned dst = lds0 + (t % PS) * 32768 + wid * 2048;
    const char* ab = abase + t * 256; const char* bb = bbase + (size_t)t * (128 * 2048);
    dma16<false>(asrc0, ab, dst);
    dma16<false>(asrc1, ab, dst + 1024);
    dma16<false>(bsrc, bb, dst + 16384);
    dma16<false>(bsrc, bb + 8 * 2048, dst + 16384 + 1024);
  };
  issue(0); issue(1); issue(2);
  const int qm = wid & 1, qn = (wid >> 1) & 1, kh = wid >> 2;
  const unsigned r32 = lane & 31, hh = lane >> 5;
  const unsigned ord = (32 * qm + r32) * 256 + (((8 * kh + hh) ^ (r32 & 15)) * 16);
  const unsigned vq = (lane & 15) >> 2;
  const unsigned wrd = 16384 + (64 * kh + 8 * hh + vq) * 128 + ((qn ^ ((vq >> 1) & 1)) * 64) + 32 * ((lane >> 4) & 1) + 8 * (lane & 3);
  f32x16 acc;
#pragma unroll
  for (int r = 0; r < 16; ++r) acc[r] = 0.f;
  for (int t = 0; t < 32; ++t) {
    if (t + 2 < 32) WAIT_BAR(8); else if (t + 2 == 32) WAIT_BAR(4); else WAIT_BAR(0);
    if (t + 3 < 32) issue(t + 3);
    const unsigned slot = (t % PS) * 32768;
#pragma unroll
    for (int s = 0; s < 4; ++s) {
      bf16x8 of = lds_rd128(lds + slot + (ord ^ (32 * s)));
      const lds_char* a = lds + slot + wrd + 2048 * s;
      bf16x8 wf = cat8(lds_tr(a), lds_tr(a + 512));
      acc = MFMA32(wf, of, acc);
    }
  }
  BAR_ONLY();
  __attribute__((address_space(3))) float* ex = (__attribute__((address_space(3))) float*)(lds + wid * 2048);
#pragma unroll
  for (int j2 = 0; j2 < 2; ++j2) {
    f32x4 v;
    if (kh == 0) v = (f32x4){acc[8 + 4 * j2], acc[9 + 4 * j2], acc[10 + 4 * j2], acc[11 + 4 * j2]};
    else         v = (f32x4){acc[4 * j2], acc[1 + 4 * j2], acc[2 + 4 * j2], acc[3 + 4 * j2]};
    *(__attribute__((address_space(3))) f32x4*)(ex + j2 * 256 + lane * 4) = v;
  }
  BAR_ONLY();
  const __attribute__((address_space(3))) float* pex = (const __attribute__((address_space(3))) float*)(lds + (wid ^ 4) * 2048);
  char* orow = out + ((size_t)(tm * 64 + 32 * qm + r32) * DM + tn * 64 + 32 * qn + 16 * kh + 4 * hh) * 2;
#pragma unroll
  for (int j2 = 0; j2 < 2; ++j2) {
    const f32x4 pv = *(const __attribute__((address_space(3))) f32x4*)(pex + j2 * 256 + lane * 4);
    float mine[4];
#pragma unroll
    for (int e = 0; e < 4; ++e) mine[e] = kh == 0 ? acc[4 * j2 + e] : acc[8 + 4 * j2 + e];
    u32x2 w = {cvtpk(mine[0] + pv[0], mine[1] + pv[1]), cvtpk(mine[2] + pv[2], mine[3] + pv[3])};
    *(u32x2*)(orow + 16 * j2) = w;
  }
}

extern "C" void kernel_launch(void* const* d_in, const int* in_sizes, int n_in,
                              void* d_out, int out_size, void* d_ws, size_t ws_size,
                              hipStream_t stream) {
  const char* x = (const char*)d_in[0];
  const char* kc = (const char*)d_in[1];
  const char* vc = (const char*)d_in[2];
  const char* wq = (const char*)d_in[3];
  const char* wo = (const char*)d_in[4];
  char* ows = (char*)d_ws;
  (void)hipFuncSetAttribute((const void*)attn_kernel, hipFuncAttributeMaxDynamicSharedMemorySize, LDS1);
  (void)hipFuncSetAttribute((const void*)oproj_kernel, hipFuncAttributeMaxDynamicSharedMemorySize, PS * 32768);
  attn_kernel<<<256, 512, LDS1, stream>>>(x, kc, vc, wq, ows);
  oproj_kernel<<<256, 512, PS * 32768, stream>>>(ows, wo, (char*)d_out);
}
```

```cpp
#include <hip/hip_runtime.h>
#include <hip/hip_bf16.h>
#include <stdint.h>

typedef float    f32x16 __attribute__((ext_vector_type(16)));
typedef float    f32x4  __attribute__((ext_vector_type(4)));
typedef float    f32x2  __attribute__((ext_vector_type(2)));
typedef __bf16   bf16x8 __attribute__((ext_vector_type(8)));
typedef __bf16   bf16x2 __attribute__((ext_vector_type(2)));
typedef short    s16x4  __attribute__((ext_vector_type(4)));
typedef short    s16x8  __attribute__((ext_vector_type(8)));
typedef unsigned u32x4  __attribute__((ext_vector_type(4)));
typedef unsigned u32x2  __attribute__((ext_vector_type(2)));
#define LAS __attribute__((address_space(3)))
typedef LAS char lds_char;

#define DI __device__ __forceinline__
#define SBAR() __builtin_amdgcn_sched_barrier(0)
#define PIN(x) asm volatile("" : "+v"(x))

constexpr int SQ = 128, DM = 1024, NH = 32, SK = 2048, DH = 128;
constexpr int NTILE = 32;
constexpr int LDS1 = 163840;
constexpr float QSCALE = 0.08838834764831845f * 1.4426950408889634f;
#define RTHR 8.0f
constexpr unsigned P0 = 0, P1 = 32768, P2 = 65536, P3 = 98304, P4 = 131072;

DI unsigned rfl(unsigned x) { return __builtin_amdgcn_readfirstlane(x); }
DI unsigned cvtpk(float lo, float hi) { f32x2 v = {lo, hi}; bf16x2 b = __builtin_convertvector(v, bf16x2); return __builtin_bit_cast(unsigned, b); }
DI float max3f(float a, float b, float c) { return __builtin_fmaxf(__builtin_fmaxf(a, b), c); }

template <bool NT> DI void dma16(unsigned voff, const void* sbase, unsigned ldsdst) {
  if constexpr (NT) asm volatile("s_mov_b32 m0, %2\n\ts_nop 0\n\tglobal_load_lds_dwordx4 %0, %1 nt" :: "v"(voff), "s"(sbase), "s"(ldsdst) : "memory");
  else              asm volatile("s_mov_b32 m0, %2\n\ts_nop 0\n\tglobal_load_lds_dwordx4 %0, %1" :: "v"(voff), "s"(sbase), "s"(ldsdst) : "memory");
}
DI bf16x8 lds_rd128(const lds_char* p) { return *(const LAS bf16x8*)p; }
DI s16x4 lds_tr(const lds_char* p) { return __builtin_amdgcn_ds_read_tr16_b64_v4i16((LAS s16x4*)p); }
DI bf16x8 cat8(s16x4 a, s16x4 b) { s16x8 r = __builtin_shufflevector(a, b, 0, 1, 2, 3, 4, 5, 6, 7); return __builtin_bit_cast(bf16x8, r); }
#define MFMA32(a, b, c) __builtin_amdgcn_mfma_f32_32x32x16_bf16(a, b, c, 0, 0, 0)
#define WAIT_BAR(N) asm volatile("s_waitcnt vmcnt(%0) lgkmcnt(0)\n\ts_barrier" :: "i"(N) : "memory")
#define BAR_ONLY() asm volatile("s_waitcnt lgkmcnt(0)\n\ts_barrier" ::: "memory")

#define STAMP(i) do {} while (0)

__global__ __launch_bounds__(512, 2)
void attn_kernel(const char* __restrict__ x, const char* __restrict__ kc, const char* __restrict__ vc,
                 const char* __restrict__ wq, char* __restrict__ ows) {
  extern __shared__ __attribute__((aligned(16))) char smem[];
  lds_char* lds = (lds_char*)smem;
  const unsigned lds0 = (unsigned)(uintptr_t)lds;
  const int tid = threadIdx.x, lane = tid & 63;
  const int wid = rfl(tid >> 6);
  const int bid = blockIdx.x;
  const int xg = bid & 7, jj = bid >> 3;
  const int b = (xg & 1) * 4 + (jj & 3), h = (xg >> 1) * 8 + (jj >> 2);
  const int bh = b * NH + h;
  STAMP(0);

  const unsigned dr = (lane >> 2) & 7, dblk = lane >> 5, dch = lane & 3;
  const unsigned kxor = (2 * wid + (dr >> 2)) & 3;
  const unsigned ksrc = (8 * wid + dr) * 256 + dblk * 64 + ((dch ^ kxor) * 16);
  const unsigned vsrc = (8 * wid + dr) * 256 + dblk * 64 + dch * 16;
  const char* kbase = kc + (size_t)bh * (SK * DH * 2);
  const char* vbase = vc + (size_t)bh * (SK * DH * 2);
  const unsigned wdst = lds0 + wid * 2048;
  auto dma_k = [&](int t, unsigned reg) { const char* s = kbase + t * 16384; dma16<true>(ksrc, s, wdst + reg); dma16<true>(ksrc, s + 128, wdst + reg + 1024); };
  auto dma_v = [&](int t, unsigned reg) { const char* s = vbase + t * 16384; dma16<true>(vsrc, s, wdst + reg); dma16<true>(vsrc, s + 128, wdst + reg + 1024); };

  const int qb = wid & 3, kh = wid >> 2;
  const unsigned r32 = lane & 31, hh = lane >> 5;
  const unsigned vq = (lane & 15) >> 2;
  bf16x8 qf[8];
  {
    const int rot = (4 * (jj & 3) + 2 * (jj >> 2)) & 15;
    const char* xb = x + (size_t)b * (SQ * DM * 2);
    const char* wb = wq + h * 256;
    const unsigned xsrc0 = (16 * wid + dr) * 2048 + dblk * 64 + ((dch ^ (dr >> 2)) * 16);
    const unsigned xsrc1 = (16 * wid + 8 + dr) * 2048 + dblk * 64 + ((dch ^ (2 + (dr >> 2))) * 16);
    const unsigned wsrc = (8 * wid + dr) * 8192 + dblk * 64 + dch * 16;
    auto issue_q = [&](int i, unsigned reg) {
      const int blk = (i + rot) & 15;
      const char* xs = xb + blk * 128; const char* ws = wb + (size_t)blk * (64 * 8192);
      dma16<false>(xsrc0, xs, wdst + reg); dma16<false>(xsrc1, xs, wdst + reg + 1024);
      dma16<true>(wsrc, ws, wdst + reg + 16384); dma16<true>(wsrc, ws + 128, wdst + reg + 16384 + 1024);
    };
    constexpr unsigned QP[3] = {P2, P1, P4};
    issue_q(0, QP[0]); issue_q(1, QP[1]);
    f32x16 qa[2];
#pragma unroll
    for (int r = 0; r < 16; ++r) { qa[0][r] = 0.f; qa[1][r] = 0.f; }
    const lds_char* xr0 = lds + (4 * qb + (r32 >> 3)) * 1024 + 64 * (r32 & 7) + 16 * (hh ^ ((r32 >> 2) & 3));
    const lds_char* xr1 = lds + (4 * qb + (r32 >> 3)) * 1024 + 64 * (r32 & 7) + 16 * ((2 + hh) ^ ((r32 >> 2) & 3));
    const lds_char* wr = lds + 16384 + 1024 * kh + 2048 * hh + 64 * vq + 32 * ((lane >> 4) & 1) + 8 * (lane & 3);
#pragma unroll
    for (int i = 0; i < 16; ++i) {
      constexpr auto kvn = [](int it) { return (it == 2 || it == 5 || it == 8 || it == 11) ? 2 : (it == 14 ? 4 : 0); };
      const int younger = (i >= 2 ? kvn(i - 2) : 0) + (i + 1 < 16 ? 4 : 0) + (i >= 1 ? kvn(i - 1) : 0);
      switch (younger) { case 0: WAIT_BAR(0); break; case 4: WAIT_BAR(4); break; case 6: WAIT_BAR(6); break; default: WAIT_BAR(0); break; }
      if (i == 0) STAMP(1);
      if (i + 2 < 16) issue_q(i + 2, QP[(i + 2) % 3]);
      if (i == 2) dma_k(0, P3);
      if (i == 5) dma_k(1, P3 + 16384);
      if (i == 8) dma_v(0, P0 + 16384);
      if (i == 11) dma_k(2, P0);
      if (i == 14) { dma_k(3, P1); dma_v(1, P1 + 16384); }
      const unsigned reg = QP[i % 3];
      bf16x8 xf[4], wf[4][2];
#pragma unroll
      for (int s = 0; s < 4; ++s) {
        xf[s] = lds_rd128(((s & 1) ? xr1 : xr0) + reg + 512 * (s >> 1));
#pragma unroll
        for (int j = 0; j < 2; ++j) wf[s][j] = cat8(lds_tr(wr + reg + 4096 * s + 512 * j), lds_tr(wr + reg + 4096 * s + 512 * j + 256));
      }
      SBAR();
#pragma unroll
      for (int s = 0; s < 4; ++s) { qa[0] = MFMA32(wf[s][0], xf[s], qa[0]); qa[1] = MFMA32(wf[s][1], xf[s], qa[1]); }
    }
    STAMP(2);
    const unsigned row = 32 * qb + r32;
    lds_char* qw = lds + P4 + 2048 * (row >> 3) + 64 * (row & 7) + 8 * hh;
#pragma unroll
    for (int j = 0; j < 2; ++j)
#pragma unroll
      for (int r4 = 0; r4 < 4; ++r4) {
        u32x2 w = {cvtpk(qa[j][4 * r4] * QSCALE, qa[j][4 * r4 + 1] * QSCALE), cvtpk(qa[j][4 * r4 + 2] * QSCALE, qa[j][4 * r4 + 3] * QSCALE)};
        *(LAS u32x2*)(qw + 512 * (2 * kh + j) + 16 * (r4 ^ ((row >> 2) & 3))) = w;
      }
    WAIT_BAR(4);
    dma_k(4, P2); dma_v(2, P2 + 16384);
    const lds_char* qr0 = lds + P4 + 2048 * (row >> 3) + 64 * (row & 7) + 16 * (hh ^ ((row >> 2) & 3));
    const lds_char* qr1 = lds + P4 + 2048 * (row >> 3) + 64 * (row & 7) + 16 * ((2 + hh) ^ ((row >> 2) & 3));
#pragma unroll
    for (int s = 0; s < 8; ++s) qf[s] = lds_rd128(((s & 1) ? qr1 : qr0) + 512 * (s >> 1));
  }

  const lds_char* kr0 = lds + 2048 * (4 * kh + (r32 >> 3)) + 64 * (r32 & 7) + 16 * (hh ^ ((r32 >> 2) & 3));
  const lds_char* kr1 = lds + 2048 * (4 * kh + (r32 >> 3)) + 64 * (r32 & 7) + 16 * ((2 + hh) ^ ((r32 >> 2) & 3));
  const lds_char* vr = lds + 16384 + 8192 * kh + 64 * (4 * hh + vq) + 32 * ((lane >> 4) & 1) + 8 * (lane & 3);
  f32x16 o[4], SA, SB, negm;
  bf16x8 kf[8];
  float mref, lsum = 0.f;
#pragma unroll
  for (int i = 0; i < 4; ++i)
#pragma unroll
    for (int r = 0; r < 16; ++r) o[i][r] = 0.f;
  {
#pragma unroll
    for (int s = 0; s < 8; ++s) kf[s] = lds_rd128(((s & 1) ? kr1 : kr0) + P3 + 512 * (s >> 1));
#pragma unroll
    for (int r = 0; r < 16; ++r) SA[r] = 0.f;
#pragma unroll
    for (int s = 0; s < 8; ++s) SA = MFMA32(kf[s], qf[s], SA);
    SBAR();
#pragma unroll
    for (int s = 0; s < 8; ++s) kf[s] = lds_rd128(((s & 1) ? kr1 : kr0) + P3 + 16384 + 512 * (s >> 1));
    float mx = fmaxf(SA[0], SA[1]);
#pragma unroll
    for (int r = 2; r < 16; ++r) mx = fmaxf(mx, SA[r]);
    auto sw = __builtin_amdgcn_permlane32_swap(__float_as_uint(mx), __float_as_uint(mx), false, false);
    mref = fmaxf(__uint_as_float(sw[0]), __uint_as_float(sw[1]));
#pragma unroll
    for (int r = 0; r < 16; ++r) { SA[r] -= mref; negm[r] = -mref; }
  }
  STAMP(3);

#define EX2(C, i) do { C[i] = __builtin_amdgcn_exp2f(C[i]); C[i + 1] = __builtin_amdgcn_exp2f(C[i + 1]); } while (0)
#define STEP(C, N, t, QK, KRD, DK, DV) do { \
    const unsigned sl_ = slot, nsl_ = slot == 0 ? P2 : slot - 32768; \
    const lds_char* vp_ = vr + sl_; const lds_char* k0_ = kr0 + sl_; const lds_char* k1_ = kr1 + sl_; \
    bf16x8 vf[8]; u32x4 pw0, pw1; \
    _Pragma("unroll") for (int g = 0; g < 8; ++g) { \
      vf[g] = cat8(lds_tr(vp_ + 4096 * (g >> 2) + 512 * (g & 3)), lds_tr(vp_ + 4096 * (g >> 2) + 512 * (g & 3) + 2048)); \
      if (QK) { if (g == 0) N = MFMA32(kf[0], qf[0], negm); else N = MFMA32(kf[g], qf[g], N); } \
      EX2(C, 2 * g); \
      if (g >= 1) { lsum += C[2 * g - 2]; lsum += C[2 * g - 1]; PIN(lsum); \
        const unsigned w_ = cvtpk(C[2 * g - 2], C[2 * g - 1]); if (g <= 4) pw0[(g - 1) & 3] = w_; else pw1[(g - 1) & 3] = w_; } \
      if (g == DMAG0) { if (DK) dma_k((t) + 4, nsl_); } \
      if (g == DMAG1) { if (DV) dma_v((t) + 2, nsl_ + 16384); } \
      PIN(C); SBAR(); \
    } \
    float mx0_; \
    _Pragma("unroll") for (int g = 0; g < 8; ++g) { \
      o[g & 3] = MFMA32(vf[g], __builtin_bit_cast(bf16x8, g < 4 ? pw0 : pw1), o[g & 3]); \
      if (g == 0) { lsum += C[14]; lsum += C[15]; pw1[3] = cvtpk(C[14], C[15]); PIN(pw1); PIN(lsum); } \
      if (KRD) kf[g] = lds_rd128(((g & 1) ? k1_ : k0_) + 512 * (g >> 1)); \
      if (QK) { \
        if (g == 2) { mx0_ = max3f(RTHR, N[0], N[1]); mx0_ = max3f(mx0_, N[2], N[3]); PIN(mx0_); } \
        if (g == 3) { mx0_ = max3f(mx0_, N[4], N[5]); mx0_ = max3f(mx0_, N[6], N[7]); PIN(mx0_); } \
        if (g == 4) { mx0_ = max3f(mx0_, N[8], N[9]); mx0_ = max3f(mx0_, N[10], N[11]); PIN(mx0_); } \
        if (g == 5) { mx0_ = max3f(mx0_, N[12], N[13]); mx0_ = max3f(mx0_, N[14], N[15]); PIN(mx0_); } \
      } \
      SBAR(); \
    } \
    if (QK) { \
      if (__builtin_expect(__any(mx0_ > RTHR), 0)) { \
        mx0_ = N[0]; _Pragma("unroll") for (int r = 1; r < 16; ++r) mx0_ = fmaxf(mx0_, N[r]); \
        auto sw_ = __builtin_amdgcn_permlane32_swap(__float_as_uint(mx0_), __float_as_uint(mx0_), false, false); \
        const float dl_ = fmaxf(fmaxf(__uint_as_float(sw_[0]), __uint_as_float(sw_[1])), 0.f); \
        const float f_ = __builtin_amdgcn_exp2f(-dl_); \
        mref += dl_; lsum *= f_; \
        _Pragma("unroll") for (int r = 0; r < 16; ++r) { N[r] -= dl_; negm[r] = -mref; } \
        _Pragma("unroll") for (int i = 0; i < 4; ++i) _Pragma("unroll") for (int r = 0; r < 16; ++r) o[i][r] *= f_; \
      } \
    } \
    slot = slot == P2 ? 0 : slot + 32768; \
  } while (0)
#define DMAG0 0
#define DMAG1 2

  unsigned slot = 0;
  STEP(SA, SB, 0, true, true, false, false);
  WAIT_BAR(4);
  STEP(SB, SA, 1, true, true, true, true);
  for (int t = 2; t < 28; t += 2) {
    WAIT_BAR(4);
    STEP(SA, SB, t, true, true, true, true);
    WAIT_BAR(4);
    STEP(SB, SA, t + 1, true, true, true, true);
  }
  WAIT_BAR(4);
  STEP(SA, SB, 28, true, true, false, true);
  WAIT_BAR(2);
  STEP(SB, SA, 29, true, true, false, true);
  WAIT_BAR(2);
  STEP(SA, SB, 30, true, false, false, false);
  WAIT_BAR(0);
  STEP(SB, SA, 31, false, false, false, false);
#undef STEP
#undef EX2

  STAMP(4);
  {
    { auto sw = __builtin_amdgcn_permlane32_swap(__float_as_uint(lsum), __float_as_uint(lsum), false, false);
      lsum = __uint_as_float(sw[0]) + __uint_as_float(sw[1]); }
    LAS float* ex = (LAS float*)(lds + P3 + wid * 8192);
    LAS float* st = (LAS float*)(lds + P1 + wid * 512);
#pragma unroll
    for (int j = 0; j < 2; ++j)
#pragma unroll
      for (int r4 = 0; r4 < 4; ++r4) {
        f32x4 v;
        if (kh == 0) v = (f32x4){o[2 + j][4 * r4], o[2 + j][4 * r4 + 1], o[2 + j][4 * r4 + 2], o[2 + j][4 * r4 + 3]};
        else         v = (f32x4){o[j][4 * r4], o[j][4 * r4 + 1], o[j][4 * r4 + 2], o[j][4 * r4 + 3]};
        *(LAS f32x4*)(ex + (j * 4 + r4) * 256 + lane * 4) = v;
      }
    st[lane] = mref; st[64 + lane] = lsum;
    BAR_ONLY();
    const int pw = wid ^ 4;
    const LAS float* pex = (const LAS float*)(lds + P3 + pw * 8192);
    const LAS float* pst = (const LAS float*)(lds + P1 + pw * 512);
    const float m2 = pst[lane], l2 = pst[64 + lane];
    const float mn = fmaxf(mref, m2);
    const float a1 = __builtin_amdgcn_exp2f(mref - mn), a2 = __builtin_amdgcn_exp2f(m2 - mn);
    const float inv = 1.0f / (lsum * a1 + l2 * a2);
    const float f1 = a1 * inv, f2 = a2 * inv;
    char* orow = ows + ((size_t)(b * SQ + 32 * qb + r32) * (NH * DH) + h * DH + 64 * kh + 8 * hh) * 2;
#pragma unroll
    for (int j = 0; j < 2; ++j) {
      u32x2 w[4];
#pragma unroll
      for (int r4 = 0; r4 < 4; ++r4) {
        const f32x4 pv = *(const LAS f32x4*)(pex + (j * 4 + r4) * 256 + lane * 4);
        float mine[4];
#pragma unroll
        for (int e = 0; e < 4; ++e) mine[e] = kh == 0 ? o[j][4 * r4 + e] : o[2 + j][4 * r4 + e];
        w[r4] = (u32x2){cvtpk(mine[0] * f1 + pv[0] * f2, mine[1] * f1 + pv[1] * f2), cvtpk(mine[2] * f1 + pv[2] * f2, mine[3] * f1 + pv[3] * f2)};
      }
#pragma unroll
      for (int k = 0; k < 4; k += 2) {
        auto s0 = __builtin_amdgcn_permlane32_swap(w[k][0], w[k + 1][0], false, false);
        auto s1 = __builtin_amdgcn_permlane32_swap(w[k][1], w[k + 1][1], false, false);
        *(u32x4*)(orow + (32 * j + 8 * k) * 2) = (u32x4){s0[0], s1[0], s0[1], s1[1]};
      }
    }
  }
}

constexpr int PS = 4;
__global__ __launch_bounds__(512, 2)
void oproj_kernel(const char* __restrict__ ows, const char* __restrict__ wo, char* __restrict__ out) {
  extern __shared__ __attribute__((aligned(16))) char smem[];
  lds_char* lds = (lds_char*)smem;
  const unsigned lds0 = (unsigned)(uintptr_t)lds;
  const int tid = threadIdx.x, lane = tid & 63;
  const int wid = rfl(tid >> 6);
  const int bid = blockIdx.x;
  STAMP(6);
  const int xcd = bid & 7, j = bid >> 3;
  const int tm = (xcd & 1) * 8 + (j & 7), tn = (xcd >> 1) * 4 + (j >> 3);
  const int rot = ((j >> 3) + (j & 7) * 4) & 31;
  const unsigned dr = (lane >> 2) & 7, dblk = lane >> 5, dch = lane & 3;
  const char* abase = ows + (size_t)tm * 64 * 8192;
  const unsigned asrc = (8 * wid + dr) * 8192 + dblk * 64 + ((dch ^ ((2 * wid + (dr >> 2)) & 3)) * 16);
  const char* bbase = wo + tn * 128;
  const unsigned bsrc = (16 * wid + dr) * 2048 + dblk * 64 + dch * 16;
  auto issue = [&](int t) {
    const unsigned dst = lds0 + (t % PS) * 32768 + wid * 2048;
    const int kb = (t + rot) & 31;
    const char* ab = abase + kb * 256; const char* bb = bbase + (size_t)kb * (128 * 2048);
    dma16<false>(asrc, ab, dst);
    dma16<false>(asrc, ab + 128, dst + 1024);
    dma16<false>(bsrc, bb, dst + 16384);
    dma16<false>(bsrc, bb + 8 * 2048, dst + 16384 + 1024);
  };
  issue(0); issue(1); issue(2);
  const int qm = wid & 1, qn = (wid >> 1) & 1, kh = wid >> 2;
  const unsigned r32 = lane & 31, hh = lane >> 5, vq = (lane & 15) >> 2;
  const lds_char* or0 = lds + 2048 * (4 * qm + (r32 >> 3)) + 1024 * kh + 64 * (r32 & 7) + 16 * (hh ^ ((r32 >> 2) & 3));
  const lds_char* or1 = lds + 2048 * (4 * qm + (r32 >> 3)) + 1024 * kh + 64 * (r32 & 7) + 16 * ((2 + hh) ^ ((r32 >> 2) & 3));
  const lds_char* wr = lds + 16384 + 8192 * kh + 1024 * hh + 512 * qn + 64 * vq + 32 * ((lane >> 4) & 1) + 8 * (lane & 3);
  f32x16 acc;
#pragma unroll
  for (int r = 0; r < 16; ++r) acc[r] = 0.f;
  bf16x8 of[4], wf[4];
  for (int t = 0; t < 32; ++t) {
    if (t + 2 < 32) WAIT_BAR(8); else if (t + 2 == 32) WAIT_BAR(4); else WAIT_BAR(0);
    const unsigned slot = (t % PS) * 32768;
    bf16x8 of2[4], wf2[4];
#pragma unroll
    for (int s = 0; s < 4; ++s) {
      of2[s] = lds_rd128(((s & 1) ? or1 : or0) + slot + 512 * (s >> 1));
      wf2[s] = cat8(lds_tr(wr + slot + 2048 * s), lds_tr(wr + slot + 2048 * s + 256));
    }
    SBAR();
    if (t > 0) {
#pragma unroll
      for (int s = 0; s < 4; ++s) acc = MFMA32(wf[s], of[s], acc);
    }
    SBAR();
    if (t + 3 < 32) issue(t + 3);
#pragma unroll
    for (int s = 0; s < 4; ++s) { of[s] = of2[s]; wf[s] = wf2[s]; }
  }
#pragma unroll
  for (int s = 0; s < 4; ++s) acc = MFMA32(wf[s], of[s], acc);
  BAR_ONLY();
  LAS float* ex = (LAS float*)(lds + wid * 2048);
#pragma unroll
  for (int j2 = 0; j2 < 2; ++j2) {
    f32x4 v;
    if (kh == 0) v = (f32x4){acc[8 + 4 * j2], acc[9 + 4 * j2], acc[10 + 4 * j2], acc[11 + 4 * j2]};
    else         v = (f32x4){acc[4 * j2], acc[1 + 4 * j2], acc[2 + 4 * j2], acc[3 + 4 * j2]};
    *(LAS f32x4*)(ex + j2 * 256 + lane * 4) = v;
  }
  BAR_ONLY();
  const LAS float* pex = (const LAS float*)(lds + (wid ^ 4) * 2048);
  char* orow = out + ((size_t)(tm * 64 + 32 * qm + r32) * DM + tn * 64 + 32 * qn + 16 * kh + 8 * hh) * 2;
  u32x2 w[2];
#pragma unroll
  for (int j2 = 0; j2 < 2; ++j2) {
    const f32x4 pv = *(const LAS f32x4*)(pex + j2 * 256 + lane * 4);
    float mine[4];
#pragma unroll
    for (int e = 0; e < 4; ++e) mine[e] = kh == 0 ? acc[4 * j2 + e] : acc[8 + 4 * j2 + e];
    w[j2] = (u32x2){cvtpk(mine[0] + pv[0], mine[1] + pv[1]), cvtpk(mine[2] + pv[2], mine[3] + pv[3])};
  }
  {
    auto s0 = __builtin_amdgcn_permlane32_swap(w[0][0], w[1][0], false, false);
    auto s1 = __builtin_amdgcn_permlane32_swap(w[0][1], w[1][1], false, false);
    *(u32x4*)orow = (u32x4){s0[0], s1[0], s0[1], s1[1]};
  }
}

extern "C" void kernel_launch(void* const* d_in, const int* in_sizes, int n_in,
                              void* d_out, int out_size, void* d_ws, size_t ws_size,
                              hipStream_t stream) {
  const char* x = (const char*)d_in[0];
  const char* kc = (const char*)d_in[1];
  const char* vc = (const char*)d_in[2];
  const char* wq = (const char*)d_in[3];
  const char* wo = (const char*)d_in[4];
  char* ows = (char*)d_ws;
  (void)hipFuncSetAttribute((const void*)attn_kernel, hipFuncAttributeMaxDynamicSharedMemorySize, LDS1);
  (void)hipFuncSetAttribute((const void*)oproj_kernel, hipFuncAttributeMaxDynamicSharedMemorySize, PS * 32768);
  attn_kernel<<<256, 512, LDS1, stream>>>(x, kc, vc, wq, ows);
  oproj_kernel<<<256, 512, PS * 32768, stream>>>(ows, wo, (char*)d_out);
}
```

```cpp
#include <hip/hip_runtime.h>
#include <hip/hip_bf16.h>
#include <stdint.h>

typedef float    f32x16 __attribute__((ext_vector_type(16)));
typedef float    f32x4  __attribute__((ext_vector_type(4)));
typedef float    f32x2  __attribute__((ext_vector_type(2)));
typedef __bf16   bf16x8 __attribute__((ext_vector_type(8)));
typedef __bf16   bf16x2 __attribute__((ext_vector_type(2)));
typedef short    s16x4  __attribute__((ext_vector_type(4)));
typedef short    s16x8  __attribute__((ext_vector_type(8)));
typedef unsigned u32x4  __attribute__((ext_vector_type(4)));
typedef unsigned u32x2  __attribute__((ext_vector_type(2)));
#define LAS __attribute__((address_space(3)))
typedef LAS char lds_char;

#define DI __device__ __forceinline__
#define SBAR() __builtin_amdgcn_sched_barrier(0)
#define PIN(x) asm volatile("" : "+v"(x))

constexpr int SQ = 128, DM = 1024, NH = 32, SK = 2048, DH = 128;
constexpr int NTILE = 32;
constexpr int LDS1 = 163840;
constexpr float QSCALE = 0.08838834764831845f * 1.4426950408889634f;
#define RTHR 8.0f
constexpr unsigned P0 = 0, P1 = 32768, P2 = 65536, P3 = 98304, P4 = 131072;

constexpr unsigned WS_O = 0, WS_P = 8u << 20, WS_F1 = 24u << 20, WS_F2 = WS_F1 + 32768, WS_E = WS_F1 + 65536, WS_END = WS_F1 + 66560;
constexpr unsigned SPIN_CAP = 1u << 21;
typedef __attribute__((address_space(1))) unsigned long long gu64;
#define RLX_AGENT __ATOMIC_RELAXED, __HIP_MEMORY_SCOPE_AGENT

DI unsigned rfl(unsigned x) { return __builtin_amdgcn_readfirstlane(x); }
DI unsigned cvtpk(float lo, float hi) { f32x2 v = {lo, hi}; bf16x2 b = __builtin_convertvector(v, bf16x2); return __builtin_bit_cast(unsigned, b); }
DI float max3f(float a, float b, float c) { return __builtin_fmaxf(__builtin_fmaxf(a, b), c); }

template <bool NT> DI void dma16(unsigned voff, const void* sbase, unsigned ldsdst) {
  if constexpr (NT) asm volatile("s_mov_b32 m0, %2\n\ts_nop 0\n\tglobal_load_lds_dwordx4 %0, %1 nt" :: "v"(voff), "s"(sbase), "s"(ldsdst) : "memory");
  else              asm volatile("s_mov_b32 m0, %2\n\ts_nop 0\n\tglobal_load_lds_dwordx4 %0, %1" :: "v"(voff), "s"(sbase), "s"(ldsdst) : "memory");
}
DI unsigned lane_id() { unsigned l; asm volatile("v_mbcnt_lo_u32_b32 %0, -1, 0\n\tv_mbcnt_hi_u32_b32 %0, -1, %0" : "=v"(l)); return l; }
struct LdsBase {
  const lds_char* p[3];
  DI LdsBase(const lds_char* q) { p[0] = q; p[1] = q + 65536; p[2] = q + 131072; PIN(p[1]); PIN(p[2]); }
  DI const lds_char* at(unsigned off) const { return p[off >> 16] + (off & 65535); }
};
DI bf16x8 lds_rd128(const lds_char* p) { return *(const LAS bf16x8*)p; }
DI s16x4 lds_tr(const lds_char* p) { return __builtin_amdgcn_ds_read_tr16_b64_v4i16((LAS s16x4*)p); }
DI bf16x8 cat8(s16x4 a, s16x4 b) { s16x8 r = __builtin_shufflevector(a, b, 0, 1, 2, 3, 4, 5, 6, 7); return __builtin_bit_cast(bf16x8, r); }
#define MFMA32(a, b, c) __builtin_amdgcn_mfma_f32_32x32x16_bf16(a, b, c, 0, 0, 0)
#define WAIT_BAR(N) asm volatile("s_waitcnt vmcnt(%0) lgkmcnt(0)\n\ts_barrier" :: "i"(N) : "memory")
#define BAR_ONLY() asm volatile("s_waitcnt lgkmcnt(0)\n\ts_barrier" ::: "memory")

#define KNT true
#define VNT false
#define KNT2 KNT
#define VNT2 VNT
#define WONT false
#define WQNT false
#define XNT false
#define QROT ((4 * (jj & 3) + 2 * (jj >> 2)) & 15)
#define STAMP(i) do {} while (0)

__global__ __launch_bounds__(512, 2)
void mha_kernel(const char* __restrict__ x, const char* __restrict__ kc, const char* __restrict__ vc,
                const char* __restrict__ wq, const char* __restrict__ wo, char* ws, char* __restrict__ out) {
  extern __shared__ __attribute__((aligned(16))) char smem[];
  lds_char* lds = (lds_char*)smem;
  const unsigned lds0 = (unsigned)(uintptr_t)lds;
  const int tid = threadIdx.x, lane = tid & 63;
  const int wid = rfl(tid >> 6);
  const int bid = blockIdx.x;
  const int xg = bid & 7, jj = bid >> 3;
  const int og = xg >> 1, oj = jj >> 2;
  const int b = (xg & 1) * 4 + (jj & 3), h = og * 8 + oj;
  const int bh = b * NH + h;
  STAMP(0);

  const unsigned dr = (lane >> 2) & 7, dblk = lane >> 5, dch = lane & 3;
  const unsigned kxor = (2 * wid + (dr >> 2)) & 3;
  const unsigned ksrc = (8 * wid + dr) * 256 + dblk * 64 + ((dch ^ kxor) * 16);
  const unsigned vsrc = (8 * wid + dr) * 256 + dblk * 64 + dch * 16;
  const char* kbase = kc + (size_t)bh * (SK * DH * 2);
  const char* vbase = vc + (size_t)bh * (SK * DH * 2);
  const unsigned wdst = lds0 + wid * 2048;
  const char* kring = kbase - (wid >> 2) * 16384;
  auto dma_k = [&](int t, unsigned reg) { const char* s = (t < 2 ? kbase : kring) + t * 16384; dma16<KNT>(ksrc, s, wdst + reg); dma16<KNT2>(ksrc, s + 128, wdst + reg + 1024); };
  auto dma_v = [&](int t, unsigned reg) { const char* s = vbase + t * 16384; dma16<VNT>(vsrc, s, wdst + reg); dma16<VNT2>(vsrc, s + 128, wdst + reg + 1024); };

  const int qb = wid & 3, kh = wid >> 2;
  const unsigned r32 = lane & 31, hh = lane >> 5;
  const unsigned vq = (lane & 15) >> 2;
  bf16x8 qf[8];
  {
    const int rot = QROT;
    const char* xb = x + (size_t)b * (SQ * DM * 2);
    const char* wb = wq + h * 256;
    const unsigned xsrc0 = (16 * wid + dr) * 2048 + dblk * 64 + ((dch ^ (dr >> 2)) * 16);
    const unsigned xsrc1 = (16 * wid + 8 + dr) * 2048 + dblk * 64 + ((dch ^ (2 + (dr >> 2))) * 16);
    const unsigned wsrc = (8 * wid + dr) * 8192 + dblk * 64 + dch * 16;
    auto issue_q1 = [&](int i, unsigned reg, int p) {
      const int blk = (i + rot) & 15;
      const char* xs = xb + blk * 128; const char* wsq = wb + (size_t)blk * (64 * 8192);
      if (p == 0) dma16<XNT>(xsrc0, xs, wdst + reg);
      if (p == 1) dma16<XNT>(xsrc1, xs, wdst + reg + 1024);
      if (p == 2) dma16<WQNT>(wsrc, wsq, wdst + reg + 16384);
      if (p == 3) dma16<WQNT>(wsrc, wsq + 128, wdst + reg + 16384 + 1024);
    };
    auto issue_q = [&](int i, unsigned reg) { issue_q1(i, reg, 0); issue_q1(i, reg, 1); issue_q1(i, reg, 2); issue_q1(i, reg, 3); };
    constexpr unsigned QP[3] = {P2, P1, P4};
    issue_q(0, QP[0]); issue_q(1, QP[1]); issue_q(2, QP[2]);
    f32x16 qa[2];
#pragma unroll
    for (int r = 0; r < 16; ++r) { qa[0][r] = 0.f; qa[1][r] = 0.f; }
    const LdsBase xr0(lds + (4 * qb + (r32 >> 3)) * 1024 + 64 * (r32 & 7) + 16 * (hh ^ ((r32 >> 2) & 3)));
    const LdsBase xr1(lds + (4 * qb + (r32 >> 3)) * 1024 + 64 * (r32 & 7) + 16 * ((2 + hh) ^ ((r32 >> 2) & 3)));
    const LdsBase wr(lds + 1024 * kh + 2048 * hh + 64 * vq + 32 * ((lane >> 4) & 1) + 8 * (lane & 3));
    auto rd_x = [&](unsigned reg, int s) { return lds_rd128(((s & 1) ? xr1 : xr0).at(reg + 512 * (s >> 1))); };
    auto rd_wq = [&](unsigned reg, int g) { const unsigned o_ = reg + 16384 + 4096 * (g >> 1) + 512 * (g & 1); return cat8(lds_tr(wr.at(o_)), lds_tr(wr.at(o_ + 256))); };
#define KV0 2
#define KV1 5
#define KV2 8
#define KV3 11
    auto kvissue = [&](int it) {
      if (it == KV0) dma_k(0, P3);
      if (it == KV1) dma_k(1, P3 + 16384);
      if (it == KV2) dma_v(0, P0 + 16384);
      if (it == KV3) dma_k(2, P0);
      if (it == 13) { dma_k(3, P1); dma_v(1, P1 + 16384); }
      if (it == 15) { dma_k(4, P2); dma_v(2, P2 + 16384);
      }
    };
    constexpr auto kvn = [](int it) { return (it == KV0 ? 2 : 0) + (it == KV1 ? 2 : 0) + (it == KV2 ? 2 : 0) + (it == KV3 ? 2 : 0) + (it == 13 ? 4 : 0); };
    kvissue(-1);
    WAIT_BAR(8 + kvn(-1));
    STAMP(1);
    bf16x8 xf[2][4], wf[2][8];
#pragma unroll
    for (int s = 0; s < 4; ++s) {
      xf[0][s] = rd_x(QP[0], s);
      wf[0][2 * s] = rd_wq(QP[0], 2 * s); wf[0][2 * s + 1] = rd_wq(QP[0], 2 * s + 1);
    }
#pragma unroll
    for (int i = 0; i < 16; ++i) {
      const int younger = (i >= 1 ? kvn(i - 2) : 0) + (i + 2 < 16 ? 4 : 0) + kvn(i - 1);
      if (i == 15) BAR_ONLY();
      else switch (younger) { case 4: WAIT_BAR(4); break; case 6: WAIT_BAR(6); break; case 8: WAIT_BAR(8); break; case 10: WAIT_BAR(10); break; case 12: WAIT_BAR(12); break; default: WAIT_BAR(0); break; }
      const unsigned reg = QP[(i + 1) % 3];
      const int c = i & 1, n = c ^ 1;
#pragma unroll
      for (int g = 0; g < 8; ++g) {
        qa[g & 1] = MFMA32(wf[c][g], xf[c][g >> 1], qa[g & 1]);
        if (i < 15) {
          if (!(g & 1)) xf[n][g >> 1] = rd_x(reg, g >> 1);
          wf[n][g] = rd_wq(reg, g);
        }
#define QSPREAD 1
        if (QSPREAD == 0) { if (g == 0 && i + 3 < 16) issue_q(i + 3, QP[i % 3]); if (g == 2) kvissue(i); }
        if (QSPREAD == 1) { if (!(g & 1) && i + 3 < 16) issue_q1(i + 3, QP[i % 3], g >> 1); if (g == 7) kvissue(i); }
        if (QSPREAD == 2) { if (g < 4 && i + 3 < 16) issue_q1(i + 3, QP[i % 3], g); if (g == 5) kvissue(i); }
        if (QSPREAD == 3) { if ((g & 1) && i + 3 < 16) issue_q1(i + 3, QP[i % 3], g >> 1); if (g == 7) kvissue(i); }
        SBAR();
      }
    }
    STAMP(2);
    const unsigned row = 32 * qb + r32;
    lds_char* qw = lds + P4 + 2048 * (row >> 3) + 64 * (row & 7) + 8 * hh;
#pragma unroll
    for (int j = 0; j < 2; ++j)
#pragma unroll
      for (int r4 = 0; r4 < 4; ++r4) {
        u32x2 w = {cvtpk(qa[j][4 * r4] * QSCALE, qa[j][4 * r4 + 1] * QSCALE), cvtpk(qa[j][4 * r4 + 2] * QSCALE, qa[j][4 * r4 + 3] * QSCALE)};
        *(LAS u32x2*)(qw + 512 * (2 * kh + j) + 16 * (r4 ^ ((row >> 2) & 3))) = w;
      }
    WAIT_BAR(8);
    const lds_char* qr0 = lds + P4 + 2048 * (row >> 3) + 64 * (row & 7) + 16 * (hh ^ ((row >> 2) & 3));
    const lds_char* qr1 = lds + P4 + 2048 * (row >> 3) + 64 * (row & 7) + 16 * ((2 + hh) ^ ((row >> 2) & 3));
#pragma unroll
    for (int s = 0; s < 8; ++s) qf[s] = lds_rd128(((s & 1) ? qr1 : qr0) + 512 * (s >> 1));
  }

  const lds_char* kr0 = lds + 2048 * (4 * kh + (r32 >> 3)) + 64 * (r32 & 7) + 16 * (hh ^ ((r32 >> 2) & 3));
  const lds_char* kr1 = lds + 2048 * (4 * kh + (r32 >> 3)) + 64 * (r32 & 7) + 16 * ((2 + hh) ^ ((r32 >> 2) & 3));
  const lds_char* vr = lds + 16384 + 8192 * kh + 64 * (4 * hh + vq) + 32 * ((lane >> 4) & 1) + 8 * (lane & 3);
  f32x16 o[4], SA, SB, negm;
  bf16x8 kf[8];
  float mref, lsum = 0.f;
#pragma unroll
  for (int i = 0; i < 4; ++i)
#pragma unroll
    for (int r = 0; r < 16; ++r) o[i][r] = 0.f;
  {
#pragma unroll
    for (int s = 0; s < 8; ++s) kf[s] = lds_rd128(((s & 1) ? kr1 : kr0) + P3 + 512 * (s >> 1));
#pragma unroll
    for (int r = 0; r < 16; ++r) SA[r] = 0.f;
#pragma unroll
    for (int s = 0; s < 8; ++s) SA = MFMA32(kf[s], qf[s], SA);
    SBAR();
#pragma unroll
    for (int s = 0; s < 8; ++s) kf[s] = lds_rd128(((s & 1) ? kr1 : kr0) + P3 + 16384 + 512 * (s >> 1));
    float mx = fmaxf(SA[0], SA[1]);
#pragma unroll
    for (int r = 2; r < 16; ++r) mx = fmaxf(mx, SA[r]);
    auto sw = __builtin_amdgcn_permlane32_swap(__float_as_uint(mx), __float_as_uint(mx), false, false);
    mref = fmaxf(__uint_as_float(sw[0]), __uint_as_float(sw[1]));
#pragma unroll
    for (int r = 0; r < 16; ++r) { SA[r] -= mref; negm[r] = -mref; }
  }
  STAMP(3);

#define XGT {0, 0, 1, 1, 2, 2, 3, 3, 4, 4, 5, 5, 6, 6, 7, 7}
#define KGT {8, 9, 10, 11, 12, 13, 14, 15}
#define MGT {10, 11, 12, 13}
#define DGT {0, 0, 2, 2}
  constexpr int XG[16] = XGT, KG[8] = KGT, MG[4] = MGT, DG[4] = DGT;
#define STEP(C, N, QK, XR, MIDW, D0, D1, D2, D3) do { \
    const unsigned sl_ = slot, nsl_ = slot == 0 ? P2 : slot - 32768; \
    const lds_char* vp_ = vr + sl_; const lds_char* k0_ = kr0 + kslot; const lds_char* k1_ = kr1 + kslot; \
    bf16x8 vf[8]; u32x4 pw[2]; float mx0_ = RTHR; \
    _Pragma("unroll") for (int g = 0; g < 16; ++g) { \
      if (g == 8) { if (kh) { MIDW; } } \
      if (g < 8) { \
        vf[g] = cat8(lds_tr(vp_ + 4096 * (g >> 2) + 512 * (g & 3)), lds_tr(vp_ + 4096 * (g >> 2) + 512 * (g & 3) + 2048)); \
        if (QK) { if (g == 0) N = MFMA32(kf[0], qf[0], negm); else N = MFMA32(kf[g & 7], qf[g & 7], N); } \
      } else o[g & 3] = MFMA32(vf[g & 7], __builtin_bit_cast(bf16x8, pw[(g >> 2) & 1]), o[g & 3]); \
      _Pragma("unroll") for (int p = 0; p < 8; ++p) if (XG[2 * p + 1] + 1 == g) { \
        lsum += C[2 * p]; lsum += C[2 * p + 1]; PIN(lsum); pw[p >> 2][p & 3] = cvtpk(C[2 * p], C[2 * p + 1]); PIN(pw[p >> 2]); } \
      _Pragma("unroll") for (int e = 0; e < 16; ++e) if (XG[e] == g) C[e] = __builtin_amdgcn_exp2f(C[e]); \
      _Pragma("unroll") for (int i = 0; i < 8; ++i) if (KG[i] == g) { \
        if (XR == 1) kf[i] = lds_rd128(((i & 1) ? k1_ : k0_) + 512 * (i >> 1)); \
        if (XR == 2) wst[i] = ld_w(i); \
        if (XR == 3 && i < 4) wst[8 + i] = ld_w(8 + i); } \
      if (QK) { _Pragma("unroll") for (int i = 0; i < 4; ++i) if (MG[i] == g) { \
        PIN(mx0_); mx0_ = max3f(mx0_, N[4 * i], N[4 * i + 1]); mx0_ = max3f(mx0_, N[4 * i + 2], N[4 * i + 3]); PIN(mx0_); } } \
      if (g == DG[0]) { D0; } if (g == DG[1]) { D1; } if (g == DG[2]) { D2; } if (g == DG[3]) { D3; } \
      if (g <= XG[15]) PIN(C); \
      SBAR(); \
    } \
    if (QK) { \
      if (__builtin_expect(__any(mx0_ > RTHR), 0)) { \
        mx0_ = N[0]; _Pragma("unroll") for (int r = 1; r < 16; ++r) mx0_ = fmaxf(mx0_, N[r]); \
        auto sw_ = __builtin_amdgcn_permlane32_swap(__float_as_uint(mx0_), __float_as_uint(mx0_), false, false); \
        const float dl_ = fmaxf(fmaxf(__uint_as_float(sw_[0]), __uint_as_float(sw_[1])), 0.f); \
        const float f_ = __builtin_amdgcn_exp2f(-dl_); \
        mref += dl_; lsum *= f_; \
          \
        asm volatile("s_nop 1\n\tv_mfma_f32_32x32x2_f32 %0, %1, %2, 0\n\ts_nop 7\n\ts_nop 7\n\ts_nop 7" : "+v"(negm) : "v"(hh ? 0.f : 1.f), "v"(-mref)); \
        _Pragma("unroll") for (int r = 0; r < 16; ++r) N[r] -= dl_; \
        _Pragma("unroll") for (int i = 0; i < 4; ++i) _Pragma("unroll") for (int r = 0; r < 16; ++r) o[i][r] *= f_; \
      } \
    } \
    slot = slot == P2 ? 0 : slot + 32768; kslot = kslot == P2 ? 0 : kslot + 32768; \
  } while (0)
#define TOPBAR(W) do { if (!kh) { W; } } while (0)
#define WB(n) WAIT_BAR(n)
#define WB46(f) do { if (f) WAIT_BAR(6); else WAIT_BAR(4); } while (0)
#define KK(t, reg) dma16<KNT>(ksrc, kring + (t) * 16384, wdst + (reg)), dma16<KNT2>(ksrc, kring + (t) * 16384 + 128, wdst + (reg) + 1024)
#define VV(t, reg) dma16<VNT>(vsrc, vbase + (t) * 16384, wdst + (reg)), dma16<VNT2>(vsrc, vbase + (t) * 16384 + 128, wdst + (reg) + 1024)
#define WW(pi, reg) dma16<WONT>(wosrc, wo_piece(pi) + wid * 16384, wdst + (reg)), dma16<WONT>(wosrc, wo_piece(pi) + wid * 16384 + 128, wdst + (reg) + 1024)
#define STEPX(...) STEP(__VA_ARGS__)

  const unsigned wosrc = dr * 2048 + dblk * 64 + dch * 16;
  auto wo_piece = [&](int pi) { return wo + ((size_t)((og * 8 + ((oj + (pi >> 1)) & 7)) * 128 + (pi & 1) * 64) * 2048 + oj * 256); };
  auto dma_w = [&](int pi, unsigned reg) { const char* s = wo_piece(pi) + wid * 16384; dma16<WONT>(wosrc, s, wdst + reg); dma16<WONT>(wosrc, s + 128, wdst + reg + 1024); };
  const __amdgpu_buffer_rsrc_t wor = __builtin_amdgcn_make_buffer_rsrc((void*)wo, 0, NH * DH * DM * 2, 0x00020000);
  auto ld_w = [&](int i) { return __builtin_amdgcn_raw_buffer_load_b128(wor, wosrc + (i & 1) * 128, (unsigned)(wo_piece(4 + (i >> 1)) - wo) + wid * 16384, 0); };
  u32x4 wst[12];

  unsigned slot = 0, kslot = kh * 32768;
#define TW 12
  STEP(SA, SB, true, 1, WB(4), , , , );
  TOPBAR(WB(4));
  STEPX(SB, SA, true, 1, WB(4), KK(5, nsl_), VV(3, nsl_ + 16384));
  bool wprev = false;
  for (int t = 2; t < 28; t += 2) {
    const bool wc = t >= TW && t < TW + 8;
    TOPBAR(WB46(wprev));
    STEPX(SA, SB, true, 1, WB46(wc), KK(t + 4, nsl_), VV(t + 2, nsl_ + 16384); if (wc) dma_w((t - TW) >> 1, P3 + ((t - TW) >> 1) * 16384));
    TOPBAR(WB46(wc));
    STEPX(SB, SA, true, 1, WB46(wc), KK(t + 5, nsl_), VV(t + 3, nsl_ + 16384));
    wprev = wc;
  }
  TOPBAR(WB(4));
  STEPX(SA, SB, true, 1, WB(4), if (kh) dma_k(32, P0), , VV(30, P0 + 16384));
  TOPBAR(WB(2));
  STEPX(SB, SA, true, 1, WB(2), VV(31, P1 + 16384), , );
  TOPBAR(WB(2));
  gu64* const fl1 = (gu64*)(ws + WS_F1), * const fl2 = (gu64*)(ws + WS_F2), * const ep = (gu64*)(ws + WS_E) + b * 16;
  const unsigned long long ep0 = __hip_atomic_load(ep, RLX_AGENT);
  unsigned long long tag;
  STEP(SA, SB, true, 2, WB(1), , , , );
  TOPBAR(WB(9));
  STEP(SB, SA, false, 3, , , , , );
#undef STEP

  STAMP(4);
  const unsigned ln = lane_id();
  const __amdgpu_buffer_rsrc_t wsr = __builtin_amdgcn_make_buffer_rsrc(ws, 0, WS_END, 0x00020000);
  const unsigned fo = (qb * 8 + 4 * kh) * 1024 + ln * 16;
  u32x4 of[8][4];
  {
    { auto sw = __builtin_amdgcn_permlane32_swap(__float_as_uint(lsum), __float_as_uint(lsum), false, false);
      lsum = __uint_as_float(sw[0]) + __uint_as_float(sw[1]); }
    LAS float* ex = (LAS float*)(lds + kh * P2 + qb * 8192);
    LAS float* st = (LAS float*)(lds + P1 + wid * 512);
#pragma unroll
    for (int j = 0; j < 2; ++j)
#pragma unroll
      for (int r4 = 0; r4 < 4; ++r4) {
        f32x4 v;
        if (kh == 0) v = (f32x4){o[2 + j][4 * r4], o[2 + j][4 * r4 + 1], o[2 + j][4 * r4 + 2], o[2 + j][4 * r4 + 3]};
        else         v = (f32x4){o[j][4 * r4], o[j][4 * r4 + 1], o[j][4 * r4 + 2], o[j][4 * r4 + 3]};
        *(LAS f32x4*)(ex + (j * 4 + r4) * 256 + ln * 4) = v;
      }
    st[ln] = mref; st[64 + ln] = lsum;
    BAR_ONLY();
    tag = (((unsigned long long)rfl((unsigned)(ep0 >> 32)) << 32) | rfl((unsigned)ep0)) + 1;
    asm volatile("" : "+s"(tag));
    const LAS float* pex = (const LAS float*)(lds + (kh ^ 1) * P2 + qb * 8192);
    const LAS float* pst = (const LAS float*)(lds + P1 + (wid ^ 4) * 512);
    const float m2 = pst[ln], l2 = pst[64 + ln];
    const float mn = fmaxf(mref, m2);
    const float a1 = __builtin_amdgcn_exp2f(mref - mn), a2 = __builtin_amdgcn_exp2f(m2 - mn);
    const float inv = 1.0f / (lsum * a1 + l2 * a2);
    const float f1 = a1 * inv, f2 = a2 * inv;
#pragma unroll
    for (int j = 0; j < 2; ++j) {
      u32x2 w[4];
#pragma unroll
      for (int r4 = 0; r4 < 4; ++r4) {
        const f32x4 pv = *(const LAS f32x4*)(pex + (j * 4 + r4) * 256 + ln * 4);
        float mine[4];
#pragma unroll
        for (int e = 0; e < 4; ++e) mine[e] = kh == 0 ? o[j][4 * r4 + e] : o[2 + j][4 * r4 + e];
        w[r4] = (u32x2){cvtpk(mine[0] * f1 + pv[0] * f2, mine[1] * f1 + pv[1] * f2), cvtpk(mine[2] * f1 + pv[2] * f2, mine[3] * f1 + pv[3] * f2)};
      }
#pragma unroll
      for (int k = 0; k < 4; k += 2) {
        auto s0 = __builtin_amdgcn_permlane32_swap(w[k][0], w[k + 1][0], false, false);
        auto s1 = __builtin_amdgcn_permlane32_swap(w[k][1], w[k + 1][1], false, false);
        const u32x4 v = {s0[0], s1[0], s0[1], s1[1]};
        __builtin_amdgcn_raw_buffer_store_b128(v, wsr, fo + (2 * j + (k >> 1)) * 1024, WS_O + bh * 32768, 16);
        of[0][2 * j + (k >> 1)] = v;
      }
    }
    BAR_ONLY();
  }
  f32x16 acc[4];
  bf16x8 af[2][4];
  const LdsBase wrA(lds + 16384 * kh + 2048 * (ln >> 5) + 64 * ((ln & 15) >> 2) + 32 * ((ln >> 4) & 1) + 8 * (ln & 3));
  constexpr unsigned WP[8] = {P3, P4, P0, P1, P2, P3, P4, P0};
  auto rd_a = [&](int G, int t) { const unsigned o_ = WP[G >> 2] + 4096 * (G & 3) + 512 * t; return cat8(lds_tr(wrA.at(o_)), lds_tr(wrA.at(o_ + 256))); };
  auto dma_w1 = [&](int s, int i) {
    dma16<WONT>(wosrc, wo_piece(2 * s + (i >> 1)) + wid * 16384 + (i & 1) * 128, wdst + WP[s] + (i >> 1) * 16384 + (i & 1) * 1024);
  };
  auto group = [&](int G, auto hook) {
#pragma unroll
    for (int t = 0; t < 4; ++t) {
      if (G < 31) af[(G + 1) & 1][t] = rd_a(G + 1, t);
      if (G == 0) acc[t] = MFMA32(af[0][t], __builtin_bit_cast(bf16x8, of[0][0]), (f32x16)(0.f));
      else acc[t] = MFMA32(af[G & 1][t], __builtin_bit_cast(bf16x8, of[G >> 2][G & 3]), acc[t]);
      hook(t);
      SBAR();
    }
  };
#pragma unroll
  for (int t = 0; t < 4; ++t) af[0][t] = rd_a(0, t);
#pragma unroll
  for (int G = 0; G < 4; ++G)
    group(G, [&](int t) { const int i = 4 * G + t; if (i < 12) *(LAS u32x4*)(lds + wid * 2048 + ln * 16 + (i >> 1) * 16384 + (i & 1) * 1024) = wst[i]; });
  WAIT_BAR(0);
  STAMP(5);
  if (wid == 0) {
    if (ln == 0) __hip_atomic_store(fl1 + bh * 16, tag, RLX_AGENT);
    const gu64* f = fl1 + (b * 32 + og * 8 + (ln & 7)) * 16;
#pragma unroll 1
    for (unsigned spins = 0; spins < SPIN_CAP; ++spins) {
      const unsigned long long v = __hip_atomic_load(f, RLX_AGENT);
      if (__all(v == tag || (ln & 7) == oj)) break;
      __builtin_amdgcn_s_sleep(1);
    }
    STAMP(6);
  } else if (wid >= 4) {
    const char* s = wo_piece(10 + ((wid >> 1) & 1)) + (wid & 1) * 65536;
    const unsigned d = lds0 + P3 + (wid & 3) * 8192;
#pragma unroll
    for (int i = 0; i < 4; ++i) { dma16<WONT>(wosrc, s + i * 16384, d + i * 2048); dma16<WONT>(wosrc, s + i * 16384 + 128, d + i * 2048 + 1024); }
  }
  BAR_ONLY();
  STAMP(7);
#pragma unroll
  for (int s = 1; s < 8; ++s)
#pragma unroll
    for (int ks = 0; ks < 4; ++ks) {
      of[s][ks] = __builtin_amdgcn_raw_buffer_load_b128(wsr, fo + ks * 1024, WS_O + (b * 32 + og * 8 + ((oj + s) & 7)) * 32768, 16);
      SBAR();
    }
#pragma unroll
  for (int G = 4; G < 32; ++G) {
    if (G == 5) STAMP(8);
    if (G == 7 || G == 11) BAR_ONLY();
    if (G == 23) WAIT_BAR(0);
    group(G, [&](int t) { if (G == 7) dma_w1(6, t); if (G == 11) dma_w1(7, t); });
  }
  STAMP(9);
  {
    LAS float* ex = (LAS float*)(lds + P1 + wid * 8192);
#pragma unroll
    for (int q = 0; q < 4; ++q)
#pragma unroll
      for (int e = 0; e < 2; ++e) {
        f32x4 v;
        if (kh == 0) v = (f32x4){acc[q][8 + 4 * e], acc[q][9 + 4 * e], acc[q][10 + 4 * e], acc[q][11 + 4 * e]};
        else         v = (f32x4){acc[q][4 * e], acc[q][1 + 4 * e], acc[q][2 + 4 * e], acc[q][3 + 4 * e]};
        *(LAS f32x4*)(ex + (q * 2 + e) * 256 + ln * 4) = v;
      }
    BAR_ONLY();
    const LAS float* pex = (const LAS float*)(lds + P1 + (wid ^ 4) * 8192);
    const unsigned lin = wid * 2048 + ln * 16;
    const unsigned pbase = WS_P + (b * 8 + oj) * 262144;
    f32x4 mine[2];
#pragma unroll
    for (int q = 0; q < 4; ++q) {
      f32x4 r[2];
#pragma unroll
      for (int e = 0; e < 2; ++e) {
        const f32x4 pv = *(const LAS f32x4*)(pex + (q * 2 + e) * 256 + ln * 4);
        if (kh == 0) r[e] = (f32x4){acc[q][4 * e], acc[q][1 + 4 * e], acc[q][2 + 4 * e], acc[q][3 + 4 * e]} + pv;
        else         r[e] = (f32x4){acc[q][8 + 4 * e], acc[q][9 + 4 * e], acc[q][10 + 4 * e], acc[q][11 + 4 * e]} + pv;
      }
      if (q == og) { mine[0] = r[0]; mine[1] = r[1]; }
      else {
        __builtin_amdgcn_raw_buffer_store_b128(__builtin_bit_cast(u32x4, r[0]), wsr, lin, pbase + (q * 4 + og) * 16384, 16);
        __builtin_amdgcn_raw_buffer_store_b128(__builtin_bit_cast(u32x4, r[1]), wsr, lin + 1024, pbase + (q * 4 + og) * 16384, 16);
      }
    }
    WAIT_BAR(0);
    STAMP(10);
    if (wid == 0) {
      if (ln == 0) __hip_atomic_store(fl2 + bh * 16, tag, RLX_AGENT);
      const gu64* f = fl2 + (b * 32 + (ln & 3) * 8 + oj) * 16;
#pragma unroll 1
      for (unsigned spins = 0; spins < SPIN_CAP; ++spins) {
        const unsigned long long v = __hip_atomic_load(f, RLX_AGENT);
        if (__all(v == tag || (ln & 3) == og)) break;
        __builtin_amdgcn_s_sleep(1);
      }
      if (h == 0 && ln == 0) __hip_atomic_store(ep, tag, RLX_AGENT);
    }
    BAR_ONLY();
    STAMP(11);
    u32x4 pl[3][2];
#pragma unroll
    for (int i = 0; i < 3; ++i) {
      const unsigned so = pbase + (og * 4 + ((og + 1 + i) & 3)) * 16384;
      pl[i][0] = __builtin_amdgcn_raw_buffer_load_b128(wsr, lin, so, 16);
      pl[i][1] = __builtin_amdgcn_raw_buffer_load_b128(wsr, lin + 1024, so, 16);
    }
    u32x2 w[2];
#pragma unroll
    for (int e = 0; e < 2; ++e) {
      const f32x4 v = ((mine[e] + __builtin_bit_cast(f32x4, pl[0][e])) + __builtin_bit_cast(f32x4, pl[1][e])) + __builtin_bit_cast(f32x4, pl[2][e]);
      w[e] = (u32x2){cvtpk(v[0], v[1]), cvtpk(v[2], v[3])};
    }
    auto s0 = __builtin_amdgcn_permlane32_swap(w[0][0], w[1][0], false, false);
    auto s1 = __builtin_amdgcn_permlane32_swap(w[0][1], w[1][1], false, false);
    *(u32x4*)(out + ((size_t)(b * SQ + 32 * qb + (ln & 31)) * DM + 128 * oj + 32 * og + 16 * kh + 8 * (ln >> 5)) * 2) = (u32x4){s0[0], s1[0], s0[1], s1[1]};
  }
}

extern "C" void kernel_launch(void* const* d_in, const int* in_sizes, int n_in,
                              void* d_out, int out_size, void* d_ws, size_t ws_size,
                              hipStream_t stream) {
  (void)hipFuncSetAttribute((const void*)mha_kernel, hipFuncAttributeMaxDynamicSharedMemorySize, LDS1);
  mha_kernel<<<256, 512, LDS1, stream>>>((const char*)d_in[0], (const char*)d_in[1], (const char*)d_in[2], (const char*)d_in[3],
                                         (const char*)d_in[4], (char*)d_ws, (char*)d_out);
}
```

```cpp
#include <hip/hip_runtime.h>
#include <hip/hip_bf16.h>
#include <stdint.h>

typedef float    f32x4  __attribute__((ext_vector_type(4)));
typedef float    f32x2  __attribute__((ext_vector_type(2)));
typedef __bf16   bf16x8 __attribute__((ext_vector_type(8)));
typedef __bf16   bf16x2 __attribute__((ext_vector_type(2)));
typedef short    s16x4  __attribute__((ext_vector_type(4)));
typedef short    s16x8  __attribute__((ext_vector_type(8)));
typedef unsigned u32x4  __attribute__((ext_vector_type(4)));
typedef unsigned u32x2  __attribute__((ext_vector_type(2)));
typedef unsigned long long u64;
#define LAS __attribute__((address_space(3)))
typedef LAS char lds_char;

#define DI __device__ __forceinline__
#define AI __attribute__((always_inline))
#define SBAR() __builtin_amdgcn_sched_barrier(0)
#define PIN(x) asm volatile("" : "+v"(x))

constexpr int SQ = 128, DM = 1024, NH = 32, SK = 2048, DH = 128;
constexpr int LDS1 = 163840;
constexpr float QSCALE = 0.08838834764831845f * 1.4426950408889634f;
#define RTHR 8.0f
constexpr unsigned P0 = 0, P1 = 32768, P2 = 65536, P3 = 98304, P4 = 131072;

constexpr unsigned WS_O = 0, WS_P = 8u << 20, WS_F1 = 24u << 20, WS_F2 = WS_F1 + 32768, WS_E = WS_F2 + 32768, WS_END = WS_E + 1024;
constexpr unsigned SPIN_CAP = 1u << 17;
typedef __attribute__((address_space(1))) u64 gu64;
#define RLX_AGENT __ATOMIC_RELAXED, __HIP_MEMORY_SCOPE_AGENT

DI unsigned rfl(unsigned x) { return __builtin_amdgcn_readfirstlane(x); }
DI unsigned cvtpk(float lo, float hi) { f32x2 v = {lo, hi}; bf16x2 b = __builtin_convertvector(v, bf16x2); return __builtin_bit_cast(unsigned, b); }
DI float max3f(float a, float b, float c) { return __builtin_fmaxf(__builtin_fmaxf(a, b), c); }
DI float rowmax4(float v) {
  auto a = __builtin_amdgcn_permlane16_swap(__float_as_uint(v), __float_as_uint(v), false, false);
  v = fmaxf(__uint_as_float(a[0]), __uint_as_float(a[1]));
  auto c = __builtin_amdgcn_permlane32_swap(__float_as_uint(v), __float_as_uint(v), false, false);
  return fmaxf(__uint_as_float(c[0]), __uint_as_float(c[1]));
}
DI float rowsum4(float v) {
  auto a = __builtin_amdgcn_permlane16_swap(__float_as_uint(v), __float_as_uint(v), false, false);
  v = __uint_as_float(a[0]) + __uint_as_float(a[1]);
  auto c = __builtin_amdgcn_permlane32_swap(__float_as_uint(v), __float_as_uint(v), false, false);
  return __uint_as_float(c[0]) + __uint_as_float(c[1]);
}
DI float max8(f32x4 a, f32x4 b) { return fmaxf(max3f(max3f(a[0], a[1], a[2]), a[3], b[0]), max3f(b[1], b[2], b[3])); }

template <bool NT> DI void dma16(unsigned voff, const void* sbase, unsigned ldsdst) {
  if constexpr (NT) asm volatile("s_mov_b32 m0, %2\n\ts_nop 0\n\tglobal_load_lds_dwordx4 %0, %1 nt" :: "v"(voff), "s"(sbase), "s"(ldsdst) : "memory");
  else              asm volatile("s_mov_b32 m0, %2\n\ts_nop 0\n\tglobal_load_lds_dwordx4 %0, %1" :: "v"(voff), "s"(sbase), "s"(ldsdst) : "memory");
}
DI void st16(u32x4 v, __amdgpu_buffer_rsrc_t r, unsigned voff, unsigned soff) {
  __builtin_amdgcn_raw_buffer_store_b128(v, r, voff, soff, 16);
  asm volatile("s_nop 1" :: "v"(v));
}
DI const char* code_ahead(unsigned off) {
  const char* pc; unsigned len;
  asm volatile("s_getpc_b64 %0\n.Lmha_pc%=:\n\ts_mov_b32 %1, .Lmha_end-.Lmha_pc%=" : "=s"(pc), "=s"(len));
  return pc + (off < len ? off : len);
}
template <int V> struct IC { static constexpr int value = V; };
template <int I, int N, class F> DI void sfor(F f) { if constexpr (I < N) { f(IC<I>{}); sfor<I + 1, N>(f); } }
DI unsigned lane_id() { unsigned l; asm volatile("v_mbcnt_lo_u32_b32 %0, -1, 0\n\tv_mbcnt_hi_u32_b32 %0, -1, %0" : "=v"(l)); return l; }
DI unsigned now10() { return (unsigned)__builtin_amdgcn_s_memrealtime(); }
struct LdsBase {
  const lds_char* p[3];
  DI LdsBase(const lds_char* q) { p[0] = q; p[1] = q + 65536; p[2] = q + 131072; PIN(p[1]); PIN(p[2]); }
  DI const lds_char* at(unsigned off) const { return p[off >> 16] + (off & 65535); }
};
DI bf16x8 lds_rd128(const lds_char* p) { return *(const LAS bf16x8*)p; }
DI s16x4 lds_tr(const lds_char* p) { return __builtin_amdgcn_ds_read_tr16_b64_v4i16((LAS s16x4*)p); }
DI bf16x8 cat8(s16x4 a, s16x4 b) { s16x8 r = __builtin_shufflevector(a, b, 0, 1, 2, 3, 4, 5, 6, 7); return __builtin_bit_cast(bf16x8, r); }
DI bf16x8 asb(u32x4 v) { return __builtin_bit_cast(bf16x8, v); }
#define MFMA16(a, b, c) __builtin_amdgcn_mfma_f32_16x16x32_bf16(a, b, c, 0, 0, 0)
#define WAIT_BAR(N) asm volatile("s_waitcnt vmcnt(%0) lgkmcnt(0)\n\ts_barrier" :: "i"(N) : "memory")
#define BAR_ONLY() asm volatile("s_waitcnt lgkmcnt(0)\n\ts_barrier" ::: "memory")
#define WAIT_VM0() asm volatile("s_waitcnt vmcnt(0)" ::: "memory")

#define KNT true
#define VNT true
#define QRG 14
#define STW 0
#define STAMP(i) do {} while (0)
#define MSTAMP(i) do {} while (0)

__global__ __launch_bounds__(512, 2)
void mha_kernel(const char* __restrict__ x, const char* __restrict__ kc, const char* __restrict__ vc,
                const char* __restrict__ wq, const char* __restrict__ wo, char* ws, char* __restrict__ out) {
  extern __shared__ __attribute__((aligned(16))) char smem[];
  lds_char* lds = (lds_char*)smem;
  const unsigned lds0 = (unsigned)(uintptr_t)lds;
  const int tid = threadIdx.x, lane = tid & 63;
  const int wid = rfl(tid >> 6);
  const int bid = blockIdx.x;
  const int xg = bid & 7, jj = bid >> 3;
  const int og = xg >> 1, oj = jj >> 2;
  const int b = (xg & 1) * 4 + (jj & 3), h = og * 8 + oj;
  const int bh = b * NH + h;
#define RROT 0
#define PERM 1
  auto posf = [](int o) { return PERM ? ((o & 1) ? ((o & 2) ? 4 : 6) + (o >> 2) : o >> 1) : o; };
  auto posi = [](int r) { return PERM ? (r < 4 ? 2 * r : (r < 6 ? 3 : 1) + 4 * (r & 1)) : r; };
  const int rot = RROT * (jj & 3);
  const int pos = oj == 0 ? 0 : posf(oj) + rot - (posf(oj) + rot > 7 ? 7 : 0);
  auto head_of = [&](int r) { return posi(r == 0 ? 0 : r - rot + (r - rot < 1 ? 7 : 0)); };
  STAMP(0);
  const unsigned tstart = now10();
#define CPF 2

  const int qb = wid & 3, kh = wid >> 2;
  const unsigned dr = (lane >> 2) & 7, dblk = lane >> 5, dch = lane & 3;
  const unsigned ksrc = (8 * wid + dr) * 256 + dblk * 64 + ((dch ^ (2 * (wid & 1))) * 16);
  const unsigned vsrc = (8 * wid + dr) * 256 + dblk * 64 + ((dch ^ (2 * (dr >> 2))) * 16);
  const char* kbase = kc + (size_t)bh * (SK * DH * 2);
  const char* vbase = vc + (size_t)bh * (SK * DH * 2);
  const unsigned wdst = lds0 + wid * 2048;
#define TROT 0
  const int trot = TROT;
  auto tile = [&](int t) { return ((t + trot) & 31) * 16384; };
  const unsigned vd0 = wdst + 16384 + kh * 1024, vd1 = wdst + 16384 + 1024 - kh * 1024;

  const unsigned g4 = lane >> 4, r16 = lane & 15, vq = r16 >> 2;
  constexpr unsigned QP[3] = {P2, P1, P4};
#define KV0 2
#define KV1 5
#define KV2 8
#define KV3 11
  if (kh) {
    const int qrot = (4 * (jj & 3) + 2 * (jj >> 2)) & 15;
    const char* xb = x + (size_t)b * (SQ * DM * 2) + qb * 65536;
    const char* wb = wq + h * 256 + qb * 131072;
    const unsigned xsrc0 = dr * 2048 + dblk * 64 + dch * 16, xsrc1 = (8 + dr) * 2048 + dblk * 64 + (dch ^ 2) * 16;
    const unsigned wsrc[2] = {dr * 8192 + dblk * 64 + dch * 16, dr * 8192 + dblk * 64 + (dch ^ 2) * 16};
    const unsigned ld = lds0 + qb * 4096;
    auto issue_q = [&](int i, unsigned reg) {
      const int blk = (i + qrot) & 15;
      const char* xs = xb + blk * 128; const char* wsq = wb + (size_t)blk * (64 * 8192);
#pragma unroll
      for (int p = 0; p < 2; ++p) {
        dma16<false>(xsrc0, xs + p * 32768, ld + reg + p * 2048);
        dma16<false>(xsrc1, xs + p * 32768, ld + reg + p * 2048 + 1024);
        dma16<false>(wsrc[p], wsq + p * 65536, ld + reg + 16384 + p * 2048);
        dma16<false>(wsrc[p], wsq + p * 65536 + 128, ld + reg + 16384 + p * 2048 + 1024);
      }
    };
    auto pf_k = [&](int t, unsigned reg) {
      const char* s = kbase + tile(t) - 8192; const char* s2 = kbase + tile(t >= 2 ? t - 1 : t);
      dma16<KNT>(ksrc, s, wdst + reg - 8192); dma16<KNT>(ksrc, s + 128, wdst + reg - 8192 + 1024);
      dma16<KNT>(ksrc, s2, wdst + reg); dma16<KNT>(ksrc, s2 + 128, wdst + reg + 1024);
    };
    auto pf_v = [&](int t, unsigned reg) {
      const char* s = vbase + tile(t);
      dma16<VNT>(vsrc, s - 8192, wdst + reg + 8192); dma16<VNT>(vsrc, s - 8192 + 128, wdst + reg + 8192 + 1024);
      dma16<VNT>(vsrc, s, vd0 + reg); dma16<VNT>(vsrc, s + 128, vd1 + reg);
    };
    auto kvissue = [&](int it) {
      if (it == KV0) pf_k(0, P3);
      if (it == KV1) pf_k(1, P3 + 16384);
      if (it == KV2) pf_v(0, P0);
      if (it == KV3) pf_k(2, P0);
      if (it == 13) { pf_k(3, P1); pf_v(1, P1); }
      if (it == 15) { pf_k(4, P2); pf_v(2, P2); }
    };
    constexpr auto kvn = [](int it) { return 4 * ((it == KV0) + (it == KV1) + (it == KV2) + (it == KV3) + 2 * (it == 13)); };
    issue_q(0, QP[0]); issue_q(1, QP[1]); issue_q(2, QP[2]);
    WAIT_BAR(16);
    sfor<0, 16>([&](auto ic) AI {
      constexpr int i = ic.value;
      if constexpr (i == 15) BAR_ONLY(); else WAIT_BAR(kvn(i - 2) + (i + 2 < 16 ? 8 : 0) + kvn(i - 1));
      if (i + 3 < 16) issue_q(i + 3, QP[i % 3]);
      kvissue(i);
    });
    WAIT_BAR(16);
  } else {
    const int qh = wid & 1, dh = wid >> 1;
    f32x4 qa[16];
#pragma unroll
    for (int i = 0; i < 16; ++i) qa[i] = (f32x4)(0.f);
    const LdsBase xr(lds + (8 * qh + (r16 >> 3)) * 1024 + 64 * (r16 & 7) + 16 * (g4 ^ ((r16 >> 3) * 2)));
    const LdsBase wr0(lds + 1024 * dh + 2048 * g4 + 64 * vq + 32 * (g4 & 1) + 8 * (lane & 3));
    const LdsBase wr1(lds + 1024 * dh + 2048 * g4 + 64 * vq + 32 * ((g4 & 1) ^ 1) + 8 * (lane & 3));
    auto rd_x = [&](unsigned reg, int g) { return lds_rd128(xr.at(reg + 512 * (g >> 2) + 2048 * (g & 3))); };
    auto rd_wq = [&](unsigned reg, int g) { const unsigned o_ = reg + 16384 + 8192 * (g >> 2) + 512 * ((g >> 1) & 1); const LdsBase& w_ = (g & 1) ? wr1 : wr0;
                                            return cat8(lds_tr(w_.at(o_)), lds_tr(w_.at(o_ + 256))); };
    BAR_ONLY();
    STAMP(1);
    bf16x8 xf[2][8], wf[2][8];
#pragma unroll
    for (int g = 0; g < 8; ++g) { xf[0][g] = rd_x(QP[0], g); wf[0][g] = rd_wq(QP[0], g); }
    sfor<0, 16>([&](auto ic) AI {
      constexpr int i = ic.value, c = i & 1, n = c ^ 1;
      constexpr unsigned reg = QP[(i + 1) % 3];
      BAR_ONLY();
      s16x4 lo_;
#pragma unroll
      for (int g = 0; g < 32; ++g) {
        const int dj = (g >> 2) & 3, qj = g & 3;
        qa[4 * dj + qj] = MFMA16(wf[c][4 * (g >> 4) + dj], xf[c][4 * (g >> 4) + qj], qa[4 * dj + qj]);
        SBAR();
        if (i < 15) {
#pragma unroll
          for (int e = 0; e < 24; ++e) if (e * QRG / 12 == g) {
            const int f = e / 3;
            const unsigned o_ = reg + 16384 + 8192 * (f >> 2) + 512 * ((f >> 1) & 1); const LdsBase& w_ = (f & 1) ? wr1 : wr0;
            if (e % 3 == 0) xf[n][f] = rd_x(reg, f);
            if (e % 3 == 1) lo_ = lds_tr(w_.at(o_));
            if (e % 3 == 2) wf[n][f] = cat8(lo_, lds_tr(w_.at(o_ + 256)));
          }
        }
        SBAR();
      }
    });
    STAMP(2);
    lds_char* qw = lds + P4 + 16384 * qh + 2048 * dh + 256 * (g4 >> 1) + 16 * r16 + 8 * (g4 & 1);
#pragma unroll
    for (int t = 0; t < 16; ++t) {
      u32x2 w = {cvtpk(qa[t][0] * QSCALE, qa[t][1] * QSCALE), cvtpk(qa[t][2] * QSCALE, qa[t][3] * QSCALE)};
      *(LAS u32x2*)(qw + 4096 * (t & 3) + 512 * (t >> 2)) = w;
    }
    BAR_ONLY();
  }
  const unsigned rrow = 2048 * (r16 >> 3) + 64 * (r16 & 7) + 16 * (g4 ^ (2 * (r16 >> 3)));
  const lds_char* kr = lds + 8192 * kh + rrow;
  const lds_char* vr0 = lds + 16384 + 8192 * kh + 2048 * (g4 >> 1) + 256 * (g4 & 1) + 64 * vq + 8 * (lane & 3) + 32 * (g4 & 1);
  const lds_char* vr1 = lds + 16384 + 8192 * kh + 2048 * (g4 >> 1) + 256 * (g4 & 1) + 64 * vq + 8 * (lane & 3) + 32 * ((g4 & 1) ^ 1);
  bf16x8 qf[8], kf[8];
  f32x4 o[8][2], SA[4], SB[4], negm[2];
  float mref[2], lsum[2] = {0.f, 0.f};
#define LSM 1
  f32x4 ls4[2] = {(f32x4)(0.f), (f32x4)(0.f)};
  u32x4 ones = (u32x4)(LSM == 2 || r16 == 0 ? 0x3f803f80u : 0u); PIN(ones);
#pragma unroll
  for (int i = 0; i < 8; ++i) { qf[i] = lds_rd128(lds + P4 + 8192 * qb + 16 * lane + 4096 * (i & 1) + 1024 * (i >> 1)); kf[i] = lds_rd128(kr + P3 + 4096 * (i & 1) + 512 * (i >> 1)); }
#pragma unroll
  for (int i = 0; i < 8; ++i) { o[i][0] = (f32x4)(0.f); o[i][1] = (f32x4)(0.f); }
#pragma unroll
  for (int g = 0; g < 16; ++g) SA[2 * (g & 1) + ((g >> 1) & 1)] = MFMA16(kf[g >> 1], qf[2 * (g >> 2) + (g & 1)], g < 4 ? (f32x4)(0.f) : SA[2 * (g & 1) + ((g >> 1) & 1)]);
  SBAR();
#pragma unroll
  for (int i = 0; i < 8; ++i) kf[i] = lds_rd128(kr + P3 + 16384 + 4096 * (i & 1) + 512 * (i >> 1));
#pragma unroll
  for (int j = 0; j < 2; ++j) {
    mref[j] = rowmax4(max8(SA[2 * j], SA[2 * j + 1]));
    SA[2 * j] -= mref[j]; SA[2 * j + 1] -= mref[j];
    negm[j] = (f32x4)(-mref[j]); PIN(negm[j]);
  }
  STAMP(3);

  auto rank_of = [&](int s) { return s == 0 ? pos : s - 1 + (s - 1 >= pos); };
  const unsigned wosrc = dr * 2048 + (dblk ^ (~og & 1)) * 64 + (dch ^ (2 * (dr >> 2))) * 16;
  auto wo_off = [&](int i) {
    return (unsigned)((og * 8 + head_of(rank_of(i >> 2))) * 128 + ((i >> 1) & 1) * 64 + wid * 8) * 2048 + oj * 256 + (((i & 1) ^ (~og >> 1)) & 1) * 128; };
  const __amdgpu_buffer_rsrc_t wor = __builtin_amdgcn_make_buffer_rsrc((void*)wo, 0, NH * DH * DM * 2, 0x00020000);
  u32x4 wl[24];
  auto ld_w = [&](int i) { wl[i] = __builtin_amdgcn_raw_buffer_load_b128(wor, wosrc, wo_off(8 + i), 0); };

#define PACE 114
#define PT0 1000
#define SPR PACE
#define PEND 29
  unsigned pdl = tstart + PT0 + (SPR * (pos * 32 + (jj & 3) * 8 + xg) >> 8);
  auto pace = [&] { if (PACE && !kh) { pdl += PACE; while ((int)(now10() - pdl) < 0) __builtin_amdgcn_s_sleep(1); } };

#define VRN 16
#define XGT {0, 1, 2, 3, 4, 5, 6, 7, 9, 10, 11, 12, 13, 14, 15, 16}
#define KGT {16, 18, 20, 22, 24, 26, 28, 30}
#define MGT {19, 20, 21, 22, 23, 24, 25, 26}
#define DGT {0, 1, 4, 5}
  constexpr int XG[16] = XGT, KG[8] = KGT, MG[8] = MGT;
  constexpr int DG[4] = DGT;
  constexpr auto pvd = [](int p) { return ((p & 8) ? 0 : 4) + (p & 3); };
  constexpr auto pvq = [](int p) { return (p >> 2) & 1; };
  unsigned slot = 0, kslot = kh * 32768;
  bf16x8 vf[8]; u32x4 pw[2];
  auto gaps = [&](auto g0, auto g1, auto qk, auto kread, f32x4 (&C)[4], f32x4 (&N)[4], float& mx, auto extra) AI {
    const lds_char* v0_ = vr0 + slot; const lds_char* v1_ = vr1 + slot; const lds_char* k_ = kr + kslot;
    s16x4 lo_;
#pragma unroll
    for (int g = g0.value; g < g1.value; ++g) {
      if (g < 16) {
        const int si = 2 * (g & 1) + ((g >> 1) & 1);
        if (qk.value) N[si] = MFMA16(kf[g >> 1], qf[2 * (g >> 2) + (g & 1)], g < 4 ? negm[g & 1] : N[si]);
        SBAR();
#pragma unroll
        for (int r = 0; r < 16; ++r) if (r * VRN / 16 == g) {
          const int db = (r >> 1) ^ 4;
          const lds_char* a_ = ((db & 1) ? v1_ : v0_) + 512 * (db >> 1) + 4096 * (r & 1);
          if (r & 1) vf[db] = cat8(lo_, lds_tr(a_)); else lo_ = lds_tr(a_);
        }
      } else {
        if (LSM && (g == 16 || g == 20)) { ls4[pvq(g - 16)] = MFMA16(asb(ones), asb(pw[pvq(g - 16)]), ls4[pvq(g - 16)]); SBAR(); }
        o[pvd(g - 16)][pvq(g - 16)] = MFMA16(vf[pvd(g - 16)], asb(pw[pvq(g - 16)]), o[pvd(g - 16)][pvq(g - 16)]);
        SBAR();
      }
#pragma unroll
      for (int e = 0; e < 16; ++e) if (XG[e] + 1 == g) {
        if (!LSM) { lsum[e >> 3] += C[e >> 2][e & 3]; PIN(lsum[e >> 3]); }
        if (e & 1) { pw[e >> 3][(e >> 1) & 3] = cvtpk(C[e >> 2][e & 2], C[e >> 2][e & 3]); PIN(pw[e >> 3]); } }
#pragma unroll
      for (int e = 0; e < 16; ++e) if (XG[e] == g) { C[e >> 2][e & 3] = __builtin_amdgcn_exp2f(C[e >> 2][e & 3]); PIN(C[e >> 2]); }
#pragma unroll
      for (int i = 0; i < 8; ++i) {
        if (kread.value && KG[i] == g) kf[i] = lds_rd128(k_ + 4096 * (i & 1) + 512 * (i >> 1));
        if (qk.value && MG[i] == g) { mx = max3f(mx, N[i >> 1][2 * (i & 1)], N[i >> 1][2 * (i & 1) + 1]); PIN(mx); } }
      extra(g);
      SBAR();
    }
  };
  auto step = [&](auto kread, f32x4 (&C)[4], f32x4 (&N)[4], auto mid, auto extra) AI {
    float mx = RTHR; PIN(mx);
    gaps(IC<0>{}, IC<16>{}, IC<1>{}, kread, C, N, mx, extra);
    if (kh) mid();
    gaps(IC<16>{}, IC<32>{}, IC<1>{}, kread, C, N, mx, extra);
    if (__builtin_expect(__any(mx > RTHR), 0)) {
#pragma unroll
      for (int j = 0; j < 2; ++j) {
        const float dl = fmaxf(rowmax4(max8(N[2 * j], N[2 * j + 1])), 0.f), f = __builtin_amdgcn_exp2f(-dl);
        mref[j] += dl; lsum[j] *= f; ls4[j][0] *= f; negm[j] -= dl;
        N[2 * j] -= dl; N[2 * j + 1] -= dl;
#pragma unroll
        for (int i = 0; i < 8; ++i) o[i][j] *= f;
      }
    }
    slot = slot == P2 ? 0 : slot + 32768; kslot = kslot == P2 ? 0 : kslot + 32768;
  };
  auto ring = [&](int g, int t, bool k, bool v) AI {
    const unsigned reg = slot == 0 ? P2 : slot - 32768;
    if (k && g == DG[0]) dma16<KNT>(ksrc, kbase + tile(t + 2 - kh), wdst + reg);
    if (k && g == DG[1]) dma16<KNT>(ksrc, kbase + tile(t + 2 - kh) + 128, wdst + reg + 1024);
    if (v && g == DG[2]) dma16<VNT>(vsrc, vbase + tile(t), vd0 + reg);
    if (v && g == DG[3]) dma16<VNT>(vsrc, vbase + tile(t) + 128, vd1 + reg);
  };
#define TOPBAR(W) do { if (!kh) { W; } } while (0)
#define WB45(f) do { if (f) WAIT_BAR(5); else WAIT_BAR(4); } while (0)
#define WLATE 0
#define TW 8
  step(IC<1>{}, SA, SB, [&] { WAIT_BAR(8); }, [&](int) AI {});
  pace(); TOPBAR(BAR_ONLY());
  step(IC<1>{}, SB, SA, [&] { WAIT_BAR(4); }, [&](int g) AI { ring(g, 3, true, true); });
  bool wprev = false;
#pragma unroll 1
  for (int t = 2; t < 28; t += 2) {
    const bool wc = t >= TW && t < TW + (CPF ? 18 : 16);
    if (t <= PEND) pace();
    TOPBAR(WB45(wprev));
    step(IC<1>{}, SA, SB, [&] { WB45(wc); }, [&](int g) AI { ring(g, t + 2, true, true);
      if (g == DG[3] + 1 && wc) { const int u = (t - TW) >> 1, i = CPF && u > 7 ? 7 : u;
        const unsigned dst = wdst + P3 + (i >> 1) * 16384 + (i & 1) * 1024;
        if (CPF && u == 7) {
          const unsigned l = lane_id();
          const char* a = CPF == 2 ? code_ahead(wid * 4096 + l * 64) : l < 32 ? wo + (wo_off(8) & ~255u) + (l >> 4) * 131072 + ((l >> 1) & 7) * 2048 + (l & 1) * 128 : code_ahead(wid * 4096 + (l - 32) * 128);
          asm volatile("s_mov_b32 m0, %1\n\ts_nop 0\n\tglobal_load_lds_dword %0, off" :: "v"(a), "s"(dst) : "memory");
        } else dma16<false>(wosrc, wo + wo_off(i), dst); } });
    if (t < PEND) pace();
    TOPBAR(WB45(wc));
    step(IC<1>{}, SB, SA, [&] { WB45(wc); }, [&](int g) AI { ring(g, t + 3, true, true); });
    wprev = wc;
  }
  MSTAMP(22);
  if (PEND > 27) pace();
  TOPBAR(WAIT_BAR(4));
  step(IC<1>{}, SA, SB, [&] { WAIT_BAR(WLATE ? 4 : 8); }, [&](int g) AI { ring(g, 30, kh, true); if (!WLATE && g >= 8 && g < 16 && !(g & 1)) ld_w((g - 8) >> 1); });
  MSTAMP(23);
  if (PEND > 28) pace();
  TOPBAR(WAIT_BAR(WLATE ? 2 : 6));
  step(IC<1>{}, SB, SA, [&] { WAIT_BAR(WLATE ? 2 : 10); }, [&](int g) AI { ring(g + DG[2], 31, false, true); if (!WLATE && g >= 8 && g < 16 && !(g & 1)) ld_w(4 + ((g - 8) >> 1)); });
  MSTAMP(24);
  if (PEND > 29) pace();
  TOPBAR(WAIT_BAR(WLATE ? 2 : 10));
  gu64* const fl1 = (gu64*)(ws + WS_F1) + (b * 4 + og) * 128, * const fl2 = (gu64*)(ws + WS_F2) + (b * 8 + oj) * 64, * const ep = (gu64*)(ws + WS_E) + b * 16;
  const unsigned ln = lane_id();
  const u64 ep0 = __hip_atomic_load(wid || ln < 16 ? ep : ln < 32 ? fl1 + (ln & 7) * 16 : fl2 + (ln & 3) * 16, RLX_AGENT);
  step(IC<0>{}, SA, SB, [&] { WAIT_BAR(WLATE ? 1 : 5); }, [&](int g) AI { if (g >= 16 && !(g & 1)) ld_w((WLATE ? 0 : 8) + ((g - 16) >> 1)); });
  MSTAMP(25);
  if (PEND > 30) pace();
  TOPBAR(WAIT_BAR(WLATE ? 9 : 13));

  const __amdgpu_buffer_rsrc_t wsr = __builtin_amdgcn_make_buffer_rsrc(ws, 0, WS_END, 0x00020000);
  const unsigned fo = wid * 4096 + ln * 16, obase = WS_O + (b * 4 + og) * (8 * 32768);
  u32x4 of[8][4];
  u64 fv;
#define W67 1
  {
    LAS f32x4* const ex = (LAS f32x4*)(lds + kh * P2 + qb * 8192 + ln * 16);
    const LAS f32x4* const pex = (const LAS f32x4*)(lds + (kh ^ 1) * P2 + qb * 8192 + ln * 16);
    LAS f32x4* const st = (LAS f32x4*)(lds + P1 + wid * 1024 + ln * 16);
    const LAS f32x4* const pst = (const LAS f32x4*)(lds + P1 + (wid ^ 4) * 1024 + ln * 16);
    constexpr int XL = LSM ? 21 : XG[15] + 2;
    float mx, f1[2], f2[2];
    f32x4 pv[8];
    f32x4 ps;
    auto factors = [&](int j) {
      const float mn = fmaxf(mref[j], ps[j]), a1 = __builtin_amdgcn_exp2f(mref[j] - mn), a2 = __builtin_amdgcn_exp2f(ps[j] - mn);
      const float inv = __builtin_amdgcn_rcpf(lsum[j] * a1 + ps[2 + j] * a2);
      f1[j] = a1 * inv; f2[j] = a2 * inv; PIN(f1[j]); PIN(f2[j]);
    };
    gaps(IC<0>{}, IC<XL>{}, IC<0>{}, IC<0>{}, SB, SA, mx, [&](int g) AI { if ((!W67 || WLATE) && g < 16 && (g & 1)) ld_w((WLATE ? 8 : 16) + (g >> 1)); });
    if (LSM) { lsum[0] = ls4[0][0]; lsum[1] = ls4[1][0]; }
    if (LSM < 2) { lsum[0] = rowsum4(lsum[0]); lsum[1] = rowsum4(lsum[1]); }
    *st = (f32x4){mref[0], mref[1], lsum[0], lsum[1]};
    SBAR();
    MSTAMP(26);
    if (kh) BAR_ONLY();
    MSTAMP(27);
    gaps(IC<XL>{}, IC<32>{}, IC<0>{}, IC<0>{}, SB, SA, mx, [&](int g) AI { const int p = g - 24;
      if (g == XL) ps = *pst;
      if (g == XL + 4 || g == XL + 5) factors(g - XL - 4);
      if (p >= 0) { ex[(2 * (p & 3) + (p >> 2)) * 64] = o[4 + (p & 3)][p >> 2]; pv[p] = pex[p * 64]; } });
    STAMP(4);
    if (!kh) { BAR_ONLY(); ps = *pst; factors(0); factors(1); }
    BAR_ONLY();
    if (!kh) {
#pragma unroll
      for (int i = 0; i < 8; ++i) pv[i] = pex[i * 64];
    }
#pragma unroll
    for (int i = 0; i < 8; ++i) pv[i] = pv[i] * f2[i & 1] + o[i >> 1][i & 1] * f1[i & 1];
    MSTAMP(13);
#pragma unroll
    for (int i = 0; i < 4; ++i) {
      const f32x4 u = pv[4 * (i >> 1) + (i & 1)], v = pv[4 * (i >> 1) + 2 + (i & 1)];
      of[0][i] = (u32x4){cvtpk(u[0], u[1]), cvtpk(u[2], u[3]), cvtpk(v[0], v[1]), cvtpk(v[2], v[3])};
      st16(of[0][i], wsr, fo + i * 1024, obase + pos * 32768);
    }
    MSTAMP(14);
    BAR_ONLY();
  }
#define NL 3
#define PD 6
#define PK0 4
#define PKF 3
#define XBAR 4
  constexpr int F0 = 16 * (8 - NL);
  constexpr auto fs = [](int F) { return F < F0 ? F >> 4 : 8 - NL + ((F - F0) >> 2) % NL; };
  constexpr auto fj = [](int F) { return F < F0 ? (F >> 3) & 1 : (F >> 1) & 1; };
  constexpr auto fn = [](int F) { return F < F0 ? F & 7 : 2 * ((F - F0) / (4 * NL)) + (F & 1); };
  f32x4 acc[8][2];
  bf16x8 af[8];
  const unsigned l4 = ln >> 4;
  const unsigned wosrc2 = ((ln >> 2) & 7) * 2048 + ((ln >> 5) ^ (~og & 1)) * 64 + ((ln & 3) ^ ((ln >> 3) & 2)) * 16;
  const lds_char* wa_ = lds + 16384 * kh + 2048 * (l4 >> 1) + 256 * (l4 & 1) + 64 * ((ln & 15) >> 2) + 8 * (ln & 3);
  const LdsBase wa0(wa_ + 32 * ((l4 ^ kh) & 1)), wa1(wa_ + 32 * ((l4 ^ kh ^ 1) & 1));
  const LdsBase ww(lds + wid * 2048 + ln * 16);
  constexpr unsigned WP[8] = {P3, P4, P0, P1, P2, P3, P4, P0};
  auto rd_h = [&](int F, int hi) { return lds_tr(((F & 1) ? wa1 : wa0).at(WP[fs(F)] + 8192 * fj(F) + 512 * (fn(F) >> 1) + 4096 * hi)); };
  auto wr_w = [&](int i) { *(LAS u32x4*)ww.at(WP[2 + (i >> 2)] + ((i >> 1) & 1) * 16384 + (i & 1) * 1024) = wl[i]; };
  auto frag = [&](int F) AI {
    s16x4 lo_;
#pragma unroll
    for (int qj = 0; qj < 2; ++qj) {
      acc[fn(F)][qj] = MFMA16(af[F & 7], asb(of[fs(F)][2 * fj(F) + qj]), F < 8 ? (f32x4)(0.f) : acc[fn(F)][qj]);
      SBAR();
      if (F + PD < 128) { if (qj == 0) lo_ = rd_h(F + PD, 0); else af[(F + PD) & 7] = cat8(lo_, rd_h(F + PD, 1)); }
      SBAR();
    }
  };
  u64 tag = (((u64)rfl((unsigned)(ep0 >> 32)) << 32) | rfl((unsigned)ep0)) + 1;
  asm volatile("" : "+s"(tag));
  int rdy = 0;
  auto look = [&](u64 v) { rdy = __builtin_ctz(~((unsigned)__builtin_amdgcn_ballot_w64(v == tag) & 255u | 1u << pos)); };
  auto peek = [&] { if (wid == 0) fv = __hip_atomic_load(fl1 + (ln & 7) * 16, RLX_AGENT); };
  auto need = [&](int s) AI {
    const int r = rank_of(s);
    if (wid == 0 && rdy <= r) {
      look(fv);
#pragma unroll 1
      for (unsigned spins = 0; rdy <= r && spins < SPIN_CAP; ++spins) look(__hip_atomic_load(fl1 + (ln & 7) * 16, RLX_AGENT));
    }
  };
  auto ld_o = [&](int i) { of[i >> 2][i & 3] = __builtin_amdgcn_raw_buffer_load_b128(wsr, fo + (i & 3) * 1024, obase + rank_of(i >> 2) * 32768, 16); };
  SBAR();
#pragma unroll
  for (int i = 0; i < 12; ++i) { if (i == PK0) { peek(); SBAR(); } wr_w(i); SBAR(); }
#pragma unroll
  for (int F = 0; F < PD; ++F) af[F] = cat8(rd_h(F, 0), rd_h(F, 1));
  SBAR();
  MSTAMP(15);
  LAS f32x4* const ex = (LAS f32x4*)(lds + P1 + wid * 4096 + ln * 16);
  const LAS f32x4* const pex = (const LAS f32x4*)(lds + P1 + (wid ^ 4) * 4096 + ln * 16);
  const unsigned lin = wid * 1024 + ln * 16;
  const unsigned pbase = WS_P + (b * 8 + oj) * 131072;
  f32x4 pv[2];
  auto send = [&](int t) {
    const f32x4 r0 = acc[2 * t][0] + pv[0], r1 = acc[2 * t][1] + pv[1];
    st16((u32x4){cvtpk(r0[0], r0[1]), cvtpk(r0[2], r0[3]), cvtpk(r1[0], r1[1]), cvtpk(r1[2], r1[3])}, wsr, lin, pbase + ((t ^ og ^ 3) * 4 + og) * 8192);
  };
  sfor<0, 128>([&](auto fc) AI {
    constexpr int F = fc.value, t = F < F0 ? -1 : (F - F0) / (4 * NL) - 1, ph = F < F0 ? -1 : (F - F0) % (4 * NL);
    if constexpr (F == 0) {
      WAIT_BAR(0);
      STAMP(5);
      if (wid == 0) { look(fv); if (ln == 0) __hip_atomic_store(fl1 + pos * 16, tag, RLX_AGENT); }
    }
    if constexpr (F % 16 == 0 && F < 64) need(F == 48 ? 7 : F / 8 + 2);
    if (F == 0) STAMP(6);
    if (F == 16) STAMP(7);
    if (F == 20) STAMP(8);
    if (F % 16 == 0 && F <= F0) asm volatile("s_barrier" ::: "memory");
    {
      constexpr auto pr = [](int f) { const int x0 = F0 + 4 * NL + XBAR;
        return f < F0 ? 3 - (f & 15) / 4 : f < x0 ? 3 - 4 * (f - F0) / (4 * NL + XBAR) : 3 - ((f - x0) % (4 * NL)) / NL; };
      if constexpr (F == 0 || pr(F) != pr(F - 1)) __builtin_amdgcn_s_setprio(pr(F));
    }
    if constexpr (F == 0) {
#pragma unroll
      for (int i = 4; i < 8; ++i) ld_o(i);
      SBAR();
    }
    if constexpr (F == 16) {
#pragma unroll
      for (int i = 12; i < 16; ++i) wr_w(i);
      SBAR();
    }
    if (t >= 0 && ph == XBAR) {
      constexpr int younger = 2 * ((F + PD < 128 ? F + PD : 128) - (F - XBAR + 2 + PD < 128 ? F - XBAR + 2 + PD : 128));
      asm volatile("s_waitcnt lgkmcnt(%0)\n\ts_barrier" :: "i"(younger) : "memory");
      pv[0] = pex[(t & 1) * 128]; pv[1] = pex[(t & 1) * 128 + 64];
    }
    frag(F);
    if constexpr (F % 16 == PKF && F < 48) peek();
    if constexpr (F >= 8 && F < 56 && !(F & 1)) ld_o(4 + F / 2);
    if constexpr (F >= 32 && F < 64 && F % 16 < 8 && (F & 1)) {
      constexpr int i = 16 + (F - 32) / 16 * 4 + (F % 16) / 2;
      if (W67) dma16<false>(wosrc2, wo + wo_off(8 + i), wdst + WP[2 + (i >> 2)] + ((i >> 1) & 1) * 16384 + (i & 1) * 1024); else wr_w(i);
    }
    if constexpr (W67 && F == 63) asm volatile("s_waitcnt vmcnt(12)" ::: "memory");
    if constexpr (W67 && F == 79) WAIT_VM0();
    if constexpr (t >= 0) {
      if (ph == 1) { ex[(t & 1) * 128] = acc[2 * t + 1][0]; ex[(t & 1) * 128 + 64] = acc[2 * t + 1][1]; }
      if (ph == XBAR + 3) send(t);
    }
    SBAR();
  });
  STAMP(9);
  {
    ex[128] = acc[7][0]; ex[192] = acc[7][1];
    WAIT_BAR(0);
    STAMP(10);
    if (wid == 0) {
      if (ln == 0) __hip_atomic_store(fl2 + og * 16, tag, RLX_AGENT);
#pragma unroll 1
      for (unsigned spins = 0; spins < SPIN_CAP; ++spins)
        if (((unsigned)__builtin_amdgcn_ballot_w64(__hip_atomic_load(fl2 + (ln & 3) * 16, RLX_AGENT) == tag) & 15u | 1u << og) == 15u) break;
      if (h == 0 && ln == 0) __hip_atomic_store(ep, tag, RLX_AGENT);
    }
    f32x4 v[2] = {acc[6][0] + pex[128], acc[6][1] + pex[192]};
    BAR_ONLY();
    STAMP(11);
    u32x4 pl[3];
#pragma unroll
    for (int i = 0; i < 3; ++i) pl[i] = __builtin_amdgcn_raw_buffer_load_b128(wsr, lin, pbase + (og * 4 + ((og + 1 + i) & 3)) * 8192, 16);
#pragma unroll
    for (int i = 0; i < 3; ++i)
#pragma unroll
      for (int e = 0; e < 2; ++e)
        v[e] += (f32x4){__uint_as_float(pl[i][2 * e] << 16), __uint_as_float(pl[i][2 * e] & 0xffff0000u),
                        __uint_as_float(pl[i][2 * e + 1] << 16), __uint_as_float(pl[i][2 * e + 1] & 0xffff0000u)};
    asm volatile(".Lmha_end:");
    auto s0 = __builtin_amdgcn_permlane16_swap(cvtpk(v[0][0], v[0][1]), cvtpk(v[1][0], v[1][1]), false, false);
    auto s1 = __builtin_amdgcn_permlane16_swap(cvtpk(v[0][2], v[0][3]), cvtpk(v[1][2], v[1][3]), false, false);
    *(u32x4*)(out + ((size_t)(b * SQ + 32 * qb + (ln & 31)) * DM + 128 * oj + 32 * og + 16 * kh + 8 * (ln >> 5)) * 2) = (u32x4){s0[0], s1[0], s0[1], s1[1]};
  }
}

extern "C" void kernel_launch(void* const* d_in, const int* in_sizes, int n_in,
                              void* d_out, int out_size, void* d_ws, size_t ws_size,
                              hipStream_t stream) {
  (void)hipFuncSetAttribute((const void*)mha_kernel, hipFuncAttributeMaxDynamicSharedMemorySize, LDS1);
  mha_kernel<<<256, 512, LDS1, stream>>>((const char*)d_in[0], (const char*)d_in[1], (const char*)d_in[2], (const char*)d_in[3],
                                         (const char*)d_in[4], (char*)d_ws, (char*)d_out);
}
```

```cpp
#include <hip/hip_runtime.h>
#include <hip/hip_bf16.h>
#include <stdint.h>

typedef float    f32x4  __attribute__((ext_vector_type(4)));
typedef float    f32x2  __attribute__((ext_vector_type(2)));
typedef __bf16   bf16x8 __attribute__((ext_vector_type(8)));
typedef __bf16   bf16x2 __attribute__((ext_vector_type(2)));
typedef short    s16x4  __attribute__((ext_vector_type(4)));
typedef short    s16x8  __attribute__((ext_vector_type(8)));
typedef unsigned u32x4  __attribute__((ext_vector_type(4)));
typedef unsigned u32x2  __attribute__((ext_vector_type(2)));
typedef unsigned long long u64;
#define LAS __attribute__((address_space(3)))
typedef LAS char lds_char;

#define DI __device__ __forceinline__
#define AI __attribute__((always_inline))
#define SBAR() __builtin_amdgcn_sched_barrier(0)
#define PIN(x) asm volatile("" : "+v"(x))

constexpr int SQ = 128, DM = 1024, NH = 32, SK = 2048, DH = 128;
constexpr int LDS1 = 163840;
constexpr float QSCALE = 0.08838834764831845f * 1.4426950408889634f;
#define RTHR 8.0f
constexpr unsigned P0 = 0, P1 = 32768, P2 = 65536, P3 = 98304, P4 = 131072;

constexpr unsigned WS_O = 0, WS_P = 8u << 20, WS_F1 = 24u << 20, WS_F2 = WS_F1 + 32768, WS_E = WS_F2 + 32768, WS_Z = WS_E + 1024, WS_END = WS_Z + 32768;
constexpr unsigned SPIN_CAP = 1u << 17;
typedef __attribute__((address_space(1))) u64 gu64;
#define RLX_AGENT __ATOMIC_RELAXED, __HIP_MEMORY_SCOPE_AGENT

DI unsigned rfl(unsigned x) { return __builtin_amdgcn_readfirstlane(x); }
DI unsigned cvtpk(float lo, float hi) { f32x2 v = {lo, hi}; bf16x2 b = __builtin_convertvector(v, bf16x2); return __builtin_bit_cast(unsigned, b); }
DI float max3f(float a, float b, float c) { return __builtin_fmaxf(__builtin_fmaxf(a, b), c); }
DI float rowmax4(float v) {
  auto a = __builtin_amdgcn_permlane16_swap(__float_as_uint(v), __float_as_uint(v), false, false);
  v = fmaxf(__uint_as_float(a[0]), __uint_as_float(a[1]));
  auto c = __builtin_amdgcn_permlane32_swap(__float_as_uint(v), __float_as_uint(v), false, false);
  return fmaxf(__uint_as_float(c[0]), __uint_as_float(c[1]));
}
DI float rowsum4(float v) {
  auto a = __builtin_amdgcn_permlane16_swap(__float_as_uint(v), __float_as_uint(v), false, false);
  v = __uint_as_float(a[0]) + __uint_as_float(a[1]);
  auto c = __builtin_amdgcn_permlane32_swap(__float_as_uint(v), __float_as_uint(v), false, false);
  return __uint_as_float(c[0]) + __uint_as_float(c[1]);
}
DI float max8(f32x4 a, f32x4 b) { return fmaxf(max3f(max3f(a[0], a[1], a[2]), a[3], b[0]), max3f(b[1], b[2], b[3])); }

template <bool NT> DI void dma16(unsigned voff, const void* sbase, unsigned ldsdst) {
  if constexpr (NT) asm volatile("s_mov_b32 m0, %2\n\ts_nop 0\n\tglobal_load_lds_dwordx4 %0, %1 nt" :: "v"(voff), "s"(sbase), "s"(ldsdst) : "memory");
  else              asm volatile("s_mov_b32 m0, %2\n\ts_nop 0\n\tglobal_load_lds_dwordx4 %0, %1" :: "v"(voff), "s"(sbase), "s"(ldsdst) : "memory");
}
DI void st16(u32x4 v, __amdgpu_buffer_rsrc_t r, unsigned voff, unsigned soff) {
  __builtin_amdgcn_raw_buffer_store_b128(v, r, voff, soff, 16);
  asm volatile("s_nop 1" :: "v"(v));
}
DI const char* code_ahead(unsigned off) {
  const char* pc; unsigned len;
  asm volatile("s_getpc_b64 %0\n.Lmha_pc%=:\n\ts_mov_b32 %1, .Lmha_end-.Lmha_pc%=" : "=s"(pc), "=s"(len));
  return pc + (off < len ? off : len);
}
template <int V> struct IC { static constexpr int value = V; };
template <int I, int N, class F> DI void sfor(F f) { if constexpr (I < N) { f(IC<I>{}); sfor<I + 1, N>(f); } }
#define SPO(n) ((n) * 64)
template <int I, int N> DI void stouch(unsigned& d, const char* a, const char* b) {
  if constexpr (I < N && I < 128) { asm volatile("s_load_dword %0, %1, %2" : "+s"(d) : "s"(I < 64 ? a : b), "i"(SPO(I & 63))); stouch<I + 1, N>(d, a, b); } }
DI void use(unsigned v) { asm volatile("" :: "v"(v)); }
DI unsigned lane_id() { unsigned l; asm volatile("v_mbcnt_lo_u32_b32 %0, -1, 0\n\tv_mbcnt_hi_u32_b32 %0, -1, %0" : "=v"(l)); return l; }
DI unsigned now10() { return (unsigned)__builtin_amdgcn_s_memrealtime(); }
struct LdsBase {
  const lds_char* p[3];
  DI LdsBase(const lds_char* q) { p[0] = q; p[1] = q + 65536; p[2] = q + 131072; PIN(p[1]); PIN(p[2]); }
  DI const lds_char* at(unsigned off) const { return p[off >> 16] + (off & 65535); }
};
DI bf16x8 lds_rd128(const lds_char* p) { return *(const LAS bf16x8*)p; }
DI s16x4 lds_tr(const lds_char* p) { return __builtin_amdgcn_ds_read_tr16_b64_v4i16((LAS s16x4*)p); }
DI bf16x8 cat8(s16x4 a, s16x4 b) { s16x8 r = __builtin_shufflevector(a, b, 0, 1, 2, 3, 4, 5, 6, 7); return __builtin_bit_cast(bf16x8, r); }
DI bf16x8 asb(u32x4 v) { return __builtin_bit_cast(bf16x8, v); }
#define MFMA16(a, b, c) __builtin_amdgcn_mfma_f32_16x16x32_bf16(a, b, c, 0, 0, 0)
#define WAIT_BAR(N) asm volatile("s_waitcnt vmcnt(%0) lgkmcnt(0)\n\ts_barrier" :: "i"(N) : "memory")
#define BAR_ONLY() asm volatile("s_waitcnt lgkmcnt(0)\n\ts_barrier" ::: "memory")
#define WAIT_VM0() asm volatile("s_waitcnt vmcnt(0)" ::: "memory")

#define KNT true
#define VNT true
#define QRG 14
#define STW 0
#define STAMP(i) do {} while (0)
#define MSTAMP(i) do {} while (0)

__global__ __launch_bounds__(512, 2)
void mha_kernel(const char* __restrict__ x, const char* __restrict__ kc, const char* __restrict__ vc,
                const char* __restrict__ wq, const char* __restrict__ wo, char* ws, char* __restrict__ out) {
  extern __shared__ __attribute__((aligned(16))) char smem[];
  lds_char* lds = (lds_char*)smem;
  const unsigned lds0 = (unsigned)(uintptr_t)lds;
  const int tid = threadIdx.x, lane = tid & 63;
  const int wid = rfl(tid >> 6);
  const int bid = blockIdx.x;
  const int xg = bid & 7, jj = bid >> 3;
  const int og = xg >> 1, oj = jj >> 2;
  const int b = (xg & 1) * 4 + (jj & 3), h = og * 8 + oj;
  const int bh = b * NH + h;
#define RROT 0
#define PERM 1
  auto posf = [](int o) { return PERM ? ((o & 1) ? ((o & 2) ? 4 : 6) + (o >> 2) : o >> 1) : o; };
  auto posi = [](int r) { return PERM ? (r < 4 ? 2 * r : (r < 6 ? 3 : 1) + 4 * (r & 1)) : r; };
  const int rot = RROT * (jj & 3);
  const int pos = oj == 0 ? 0 : posf(oj) + rot - (posf(oj) + rot > 7 ? 7 : 0);
  auto head_of = [&](int r) { return posi(r == 0 ? 0 : r - rot + (r - rot < 1 ? 7 : 0)); };
  STAMP(0);
  const unsigned tstart = now10();
#define CPF 2

  const int qb = wid & 3, kh = wid >> 2;
  const unsigned dr = (lane >> 2) & 7, dblk = lane >> 5, dch = lane & 3;
  const unsigned ksrc = (8 * wid + dr) * 256 + dblk * 64 + ((dch ^ (2 * (wid & 1))) * 16);
  const unsigned vsrc = (8 * wid + dr) * 256 + dblk * 64 + ((dch ^ (2 * (dr >> 2))) * 16);
  const char* kbase = kc + (size_t)bh * (SK * DH * 2);
  const char* vbase = vc + (size_t)bh * (SK * DH * 2);
  const unsigned wdst = lds0 + wid * 2048;
#define TROT 0
  const int trot = TROT;
  auto tile = [&](int t) { return ((t + trot) & 31) * 16384; };
  const unsigned vd0 = wdst + 16384 + kh * 1024, vd1 = wdst + 16384 + 1024 - kh * 1024;

  const unsigned g4 = lane >> 4, r16 = lane & 15, vq = r16 >> 2;
  constexpr unsigned QP[3] = {P2, P1, P4};
#define S2L 1
#define KV0 2
#define KV1 5
#define KV2 8
#define KV3 11
#define CP0 0
  const char* code0 = CP0 ? code_ahead(qb * (4096 * CP0) + lane * 64) : nullptr;
  if (kh) {
#define QA 8
#define QB 0
#define QC 2
#define QD 1
    const int qrot = (QA * (jj & 3) + QB * (jj >> 2) + QC * (xg >> 1) + QD * (xg & 1)) & 15;
    const char* xb = x + (size_t)b * (SQ * DM * 2) + qb * 65536;
    const char* wb = wq + h * 256 + qb * 131072;
    const unsigned xsrc0 = dr * 2048 + dblk * 64 + dch * 16, xsrc1 = (8 + dr) * 2048 + dblk * 64 + (dch ^ 2) * 16;
    const unsigned wsrc[2] = {dr * 8192 + dblk * 64 + dch * 16, dr * 8192 + dblk * 64 + (dch ^ 2) * 16};
    const unsigned ld = lds0 + qb * 4096;
    auto issue_q = [&](int i, unsigned reg) {
      const int blk = (i + qrot) & 15;
      const char* xs = xb + blk * 128; const char* wsq = wb + (size_t)blk * (64 * 8192);
#pragma unroll
      for (int p = 0; p < 2; ++p) {
        dma16<false>(xsrc0, xs + p * 32768, ld + reg + p * 2048);
        dma16<false>(xsrc1, xs + p * 32768, ld + reg + p * 2048 + 1024);
        dma16<false>(wsrc[p], wsq + p * 65536, ld + reg + 16384 + p * 2048);
        dma16<false>(wsrc[p], wsq + p * 65536 + 128, ld + reg + 16384 + p * 2048 + 1024);
      }
    };
    auto pf_k = [&](int t, unsigned reg, int p) {
      dma16<KNT>(ksrc, kbase + (p < 2 ? tile(t) - 8192 : tile(t >= 2 ? t - 1 : t)) + 128 * (p & 1), wdst + reg - (p < 2 ? 8192 : 0) + 1024 * (p & 1)); };
    auto pf_v = [&](int t, unsigned reg, int p) {
      dma16<VNT>(vsrc, vbase + tile(t) - (p < 2 ? 8192 : 0) + 128 * (p & 1), p < 2 ? wdst + reg + 8192 + 1024 * p : (p & 1 ? vd1 : vd0) + reg); };
#define KVT {KV0, KV0, KV0, KV0, KV1, KV1, KV1, KV1, KV2, KV2, KV2, KV2, KV3, KV3, KV3, KV3, 13, 13, 13, 13, 13, 13, 13, 13}
    constexpr int KVI[24] = KVT;
    auto kvissue = [&](int it) {
#pragma unroll
      for (int j = 0; j < (S2L ? 24 : 32); ++j) if (it == (j < 24 ? KVI[j] : 15)) {
        const int u = j >> 2;
        if (u == 2 || u == 5 || u == 7) pf_v(u / 2 - 1, u == 2 ? P0 : u == 5 ? P1 : P2, j & 3);
        else pf_k(u - (u > 2) - (u > 5), u == 0 ? P3 : u == 1 ? P3 + 16384 : u == 3 ? P0 : u == 4 ? P1 : P2, j & 3);
      }
    };
    constexpr auto kvn = [](int it) { constexpr int T[24] = KVT; int n = 0; for (int j = 0; j < 24; ++j) n += T[j] == it; return n; };
#define NS 0
#define SP0 0
#define SPS 2
    const char* spk = kbase + (SPS + 1) * 16384 + 8192 + qb * 4096; const char* spv = vbase + SPS * 16384 + qb * 4096;
    unsigned dummy = 0;
    issue_q(0, QP[0]);
    issue_q(1, QP[1]); issue_q(2, QP[2]);
    WAIT_BAR(16);
    sfor<0, 16>([&](auto ic) AI {
      constexpr int i = ic.value;
#define VM_BAR(N) asm volatile("s_waitcnt vmcnt(%0)\n\ts_barrier" :: "i"(N) : "memory")
      VM_BAR(i == 15 ? 63 : kvn(i - 2) + (i + 2 < 16 ? 8 : 0) + kvn(i - 1));
      if (i + 3 < 16) issue_q(i + 3, QP[i % 3]);
      kvissue(i);
      if constexpr (i >= SP0) stouch<(i - SP0) * NS, (i - SP0 + 1) * NS>(dummy, spk, spv);
    });
    WAIT_BAR(S2L ? 8 : 16);
    asm volatile("" :: "s"(dummy));
  } else {
    const int qh = wid & 1, dh = wid >> 1;
    {
      unsigned t_[CP0 + 1];
#pragma unroll
      for (int i = 0; i < CP0; ++i) t_[i] = *(const __attribute__((address_space(1))) unsigned*)(code0 + 4096 * i);
#pragma unroll
      for (int i = 0; i < CP0; ++i) asm volatile("" :: "v"(t_[i]));
    }
    f32x4 qa[16];
#pragma unroll
    for (int i = 0; i < 16; ++i) qa[i] = (f32x4)(0.f);
    const LdsBase xr(lds + (8 * qh + (r16 >> 3)) * 1024 + 64 * (r16 & 7) + 16 * (g4 ^ ((r16 >> 3) * 2)));
    const LdsBase wr0(lds + 1024 * dh + 2048 * g4 + 64 * vq + 32 * (g4 & 1) + 8 * (lane & 3));
    const LdsBase wr1(lds + 1024 * dh + 2048 * g4 + 64 * vq + 32 * ((g4 & 1) ^ 1) + 8 * (lane & 3));
    auto rd_x = [&](unsigned reg, int g) { return lds_rd128(xr.at(reg + 512 * (g >> 2) + 2048 * (g & 3))); };
    auto rd_wq = [&](unsigned reg, int g) { const unsigned o_ = reg + 16384 + 8192 * (g >> 2) + 512 * ((g >> 1) & 1); const LdsBase& w_ = (g & 1) ? wr1 : wr0;
                                            return cat8(lds_tr(w_.at(o_)), lds_tr(w_.at(o_ + 256))); };
    BAR_ONLY();
    STAMP(1);
    bf16x8 xf[2][8], wf[2][8];
#pragma unroll
    for (int g = 0; g < 8; ++g) { xf[0][g] = rd_x(QP[0], g); wf[0][g] = rd_wq(QP[0], g); }
    sfor<0, 16>([&](auto ic) AI {
      constexpr int i = ic.value, c = i & 1, n = c ^ 1;
      constexpr unsigned reg = QP[(i + 1) % 3];
      BAR_ONLY();
      s16x4 lo_;
#pragma unroll
      for (int g = 0; g < 32; ++g) {
        const int dj = (g >> 2) & 3, qj = g & 3;
        qa[4 * dj + qj] = MFMA16(wf[c][4 * (g >> 4) + dj], xf[c][4 * (g >> 4) + qj], qa[4 * dj + qj]);
        SBAR();
        if (i < 15) {
#pragma unroll
          for (int e = 0; e < 24; ++e) if (e * QRG / 12 == g) {
            const int f = e / 3;
            const unsigned o_ = reg + 16384 + 8192 * (f >> 2) + 512 * ((f >> 1) & 1); const LdsBase& w_ = (f & 1) ? wr1 : wr0;
            if (e % 3 == 0) xf[n][f] = rd_x(reg, f);
            if (e % 3 == 1) lo_ = lds_tr(w_.at(o_));
            if (e % 3 == 2) wf[n][f] = cat8(lo_, lds_tr(w_.at(o_ + 256)));
          }
        }
        SBAR();
      }
    });
    STAMP(2);
    lds_char* qw = lds + P4 + 16384 * qh + 2048 * dh + 256 * (g4 >> 1) + 16 * r16 + 8 * (g4 & 1);
#pragma unroll
    for (int t = 0; t < 16; ++t) {
      u32x2 w = {cvtpk(qa[t][0] * QSCALE, qa[t][1] * QSCALE), cvtpk(qa[t][2] * QSCALE, qa[t][3] * QSCALE)};
      *(LAS u32x2*)(qw + 4096 * (t & 3) + 512 * (t >> 2)) = w;
    }
    BAR_ONLY();
  }
  const unsigned rrow = 2048 * (r16 >> 3) + 64 * (r16 & 7) + 16 * (g4 ^ (2 * (r16 >> 3)));
  const lds_char* kr = lds + 8192 * kh + rrow;
  const lds_char* vr0 = lds + 16384 + 8192 * kh + 2048 * (g4 >> 1) + 256 * (g4 & 1) + 64 * vq + 8 * (lane & 3) + 32 * (g4 & 1);
  const lds_char* vr1 = lds + 16384 + 8192 * kh + 2048 * (g4 >> 1) + 256 * (g4 & 1) + 64 * vq + 8 * (lane & 3) + 32 * ((g4 & 1) ^ 1);
  bf16x8 qf[8], kf[8];
  f32x4 o[8][2], SA[4], SB[4], negm[2];
  float mref[2], lsum[2] = {0.f, 0.f};
#define LSM 1
  f32x4 ls4[2] = {(f32x4)(0.f), (f32x4)(0.f)};
  u32x4 ones = (u32x4)(LSM == 2 || r16 == 0 ? 0x3f803f80u : 0u); PIN(ones);
#pragma unroll
  for (int i = 0; i < 8; ++i) { qf[i] = lds_rd128(lds + P4 + 8192 * qb + 16 * lane + 4096 * (i & 1) + 1024 * (i >> 1)); kf[i] = lds_rd128(kr + P3 + 4096 * (i & 1) + 512 * (i >> 1)); }
#pragma unroll
  for (int i = 0; i < 8; ++i) { o[i][0] = (f32x4)(0.f); o[i][1] = (f32x4)(0.f); }
#pragma unroll
  for (int g = 0; g < 16; ++g) SA[2 * (g & 1) + ((g >> 1) & 1)] = MFMA16(kf[g >> 1], qf[2 * (g >> 2) + (g & 1)], g < 4 ? (f32x4)(0.f) : SA[2 * (g & 1) + ((g >> 1) & 1)]);
  SBAR();
#pragma unroll
  for (int i = 0; i < 8; ++i) kf[i] = lds_rd128(kr + P3 + 16384 + 4096 * (i & 1) + 512 * (i >> 1));
  auto piece = [&](int p, int t, unsigned reg) AI {
    if (p < 2) dma16<KNT>(ksrc, kbase + tile(t + 2 - kh) + 128 * p, wdst + reg + 1024 * p);
    else dma16<VNT>(vsrc, vbase + tile(t) + 128 * (p & 1), (p & 1 ? vd1 : vd0) + reg);
  };
  if (S2L == 2) { SBAR(); piece(0, 2, P2); piece(1, 2, P2); piece(2, 2, P2); piece(3, 2, P2); SBAR(); }
#pragma unroll
  for (int j = 0; j < 2; ++j) {
    mref[j] = rowmax4(max8(SA[2 * j], SA[2 * j + 1]));
    SA[2 * j] -= mref[j]; SA[2 * j + 1] -= mref[j];
    negm[j] = (f32x4)(-mref[j]); PIN(negm[j]);
  }
  STAMP(3);

  auto rank_of = [&](int s) { return s == 0 ? pos : s - 1 + (s - 1 >= pos); };
  const unsigned wosrc = dr * 2048 + (dblk ^ (~og & 1)) * 64 + (dch ^ (2 * (dr >> 2))) * 16;
  auto wo_off = [&](int i) {
    return (unsigned)((og * 8 + head_of(rank_of(i >> 2))) * 128 + ((i >> 1) & 1) * 64 + wid * 8) * 2048 + oj * 256 + (((i & 1) ^ (~og >> 1)) & 1) * 128; };
  const __amdgpu_buffer_rsrc_t wor = __builtin_amdgcn_make_buffer_rsrc((void*)wo, 0, NH * DH * DM * 2, 0x00020000);
  u32x4 wl[24];
  auto ld_w = [&](int i) { wl[i] = __builtin_amdgcn_raw_buffer_load_b128(wor, wosrc, wo_off(8 + i), 0); };

#define PACE 114
#define PT0 1000
#define SPR PACE
#define PEND 29
#define PHT 0x10765432
  unsigned pdl = tstart + PT0 + (SPR * (((PHT >> 4 * pos) & 15) * 32 + (jj & 3) * 8 + xg) >> 8);
#define FB0 32
#define FM 40
#define FA 2
#define FD 0
#define RA (1 << 30)
#define PW0 1
#define LEAD 150
#define POSTER ((oj & 3) == 1)
  unsigned org = tstart, win = 0; int late = 0;
  const u32x4 zr = {(unsigned)(uintptr_t)(ws + WS_Z), (unsigned)((uintptr_t)(ws + WS_Z) >> 32), 32768u, 0x00020000u};
  const int fm = POSTER ? FM : 1 << 30;
  if (FB0 < 32) asm volatile("v_lshlrev_b32 v253, 3, %0" :: "v"(lane) : "v253");
  auto look_z = [&] { if (wid == 0) { win = pdl + late >> 8;
    asm volatile("buffer_load_dwordx2 v[254:255], v253, %0, %1 offen sc1" :: "s"(zr), "s"((win & 63) * 512) : "v254", "v255"); } };
  auto adopt = [&] { if (wid == 0) {
    unsigned z, w; asm volatile("v_mov_b32 %0, v254\n\tv_mov_b32 %1, v255" : "=v"(z), "=v"(w));
    z -= org; z = w == win && z < 8192 ? z : 0;
    z = max(z, (unsigned)__builtin_amdgcn_update_dpp(0, z, 0x111, 15, 15, true)); z = max(z, (unsigned)__builtin_amdgcn_update_dpp(0, z, 0x112, 15, 15, true));
    z = max(z, (unsigned)__builtin_amdgcn_update_dpp(0, z, 0x114, 15, 15, true)); z = max(z, (unsigned)__builtin_amdgcn_update_dpp(0, z, 0x118, 15, 15, true));
    z = max(z, (unsigned)__builtin_amdgcn_update_dpp(0, z, 0x142, 15, 15, true)); z = max(z, (unsigned)__builtin_amdgcn_update_dpp(0, z, 0x143, 15, 15, true));
    z = __builtin_amdgcn_readlane(z, 63); org += z; pdl += z; } };
  auto post = [&] { const unsigned w = pdl + late + LEAD + 255 >> 8;
    asm volatile("v_mov_b32 v250, %1\n\tv_mov_b32 v251, %2\n\ts_mov_b64 exec, 1\n\tbuffer_store_dwordx2 v[250:251], off, %0, %3 sc1\n\ts_mov_b64 exec, -1"
                 :: "s"(zr), "s"(org + (late - FM >> FA) + FD), "s"(w), "s"((w & 63) * 512 + ((bid & 31) + (bid >> 7) * 32) * 8) : "v250", "v251"); };
  int ra = RA;
  auto pace = [&] { if (PACE && (PW0 ? wid == 0 : !kh)) { pdl += PACE; while ((late = now10() - pdl) < 0) __builtin_amdgcn_s_sleep(1);
    if (RA < (1 << 30) && late >= ra) { const unsigned d = (unsigned)late / PACE * PACE; pdl += d; org += d; late -= d; ra = PACE; } } };

#define VRN 16
#define XGT {0, 1, 2, 3, 4, 5, 6, 7, 9, 10, 11, 12, 13, 14, 15, 16}
#define KGT {16, 18, 20, 22, 24, 26, 28, 30}
#define MGT {19, 20, 21, 22, 23, 24, 25, 26}
#define DGT {0, 1, 4, 5}
  constexpr int XG[16] = XGT, KG[8] = KGT, MG[8] = MGT;
  constexpr int DG[4] = DGT;
  constexpr auto pvd = [](int p) { return ((p & 8) ? 0 : 4) + (p & 3); };
  constexpr auto pvq = [](int p) { return (p >> 2) & 1; };
#define XB 0
  unsigned slot = 0, kslot = kh * 32768, prev = P2, wrap = 0;
  bf16x8 vf[8]; u32x4 pw[2];
  auto gaps = [&](auto g0, auto g1, auto qk, auto kread, f32x4 (&C)[4], f32x4 (&N)[4], float& mx, auto extra) AI {
    const lds_char* v0_ = vr0 + slot; const lds_char* v1_ = vr1 + slot; const lds_char* k_ = kr + kslot;
    s16x4 lo_;
#pragma unroll
    for (int g = g0.value; g < g1.value; ++g) {
      if (g < 16) {
        const int si = 2 * (g & 1) + ((g >> 1) & 1);
        if (qk.value) N[si] = MFMA16(kf[g >> 1], qf[2 * (g >> 2) + (g & 1)], g < 4 ? negm[g & 1] : N[si]);
        SBAR();
#pragma unroll
        for (int r = 0; r < 16; ++r) if (r * VRN / 16 == g) {
          const int db = (r >> 1) ^ 4;
          const lds_char* a_ = ((db & 1) ? v1_ : v0_) + 512 * (db >> 1) + 4096 * (r & 1);
          if (r & 1) vf[db] = cat8(lo_, lds_tr(a_)); else lo_ = lds_tr(a_);
        }
      } else {
        if (LSM && (g == 16 || g == 20)) { ls4[pvq(g - 16)] = MFMA16(asb(ones), asb(pw[pvq(g - 16)]), ls4[pvq(g - 16)]); SBAR(); }
        o[pvd(g - 16)][pvq(g - 16)] = MFMA16(vf[pvd(g - 16)], asb(pw[pvq(g - 16)]), o[pvd(g - 16)][pvq(g - 16)]);
        SBAR();
      }
#pragma unroll
      for (int e = 0; e < 16; ++e) if (XG[e] + 1 == g) {
        if (!LSM) { lsum[e >> 3] += C[e >> 2][e & 3]; PIN(lsum[e >> 3]); }
        if (e & 1) { pw[e >> 3][(e >> 1) & 3] = cvtpk(C[e >> 2][e & 2], C[e >> 2][e & 3]); PIN(pw[e >> 3]); } }
#pragma unroll
      for (int e = 0; e < 16; ++e) if (XG[e] == g) { C[e >> 2][e & 3] = __builtin_amdgcn_exp2f(C[e >> 2][e & 3]); PIN(C[e >> 2]); }
#pragma unroll
      for (int i = 0; i < 8; ++i) {
        if (kread.value && KG[i] == g) kf[i] = lds_rd128(k_ + 4096 * (i & 1) + 512 * (i >> 1));
        if (qk.value && MG[i] == g) { mx = max3f(mx, N[i >> 1][2 * (i & 1)], N[i >> 1][2 * (i & 1) + 1]); PIN(mx); } }
      extra(g);
      SBAR();
    }
  };
  auto step = [&](auto kread, f32x4 (&C)[4], f32x4 (&N)[4], auto mid, auto extra) AI {
    float mx = RTHR; PIN(mx);
    gaps(IC<0>{}, IC<16>{}, IC<1>{}, kread, C, N, mx, extra);
    if (kh) mid();
    gaps(IC<16>{}, IC<32>{}, IC<1>{}, kread, C, N, mx, extra);
    if (__builtin_expect(__any(mx > RTHR), 0)) {
#pragma unroll
      for (int j = 0; j < 2; ++j) {
        const float dl = fmaxf(rowmax4(max8(N[2 * j], N[2 * j + 1])), 0.f), f = __builtin_amdgcn_exp2f(-dl);
        mref[j] += dl; lsum[j] *= f; ls4[j][0] *= f; negm[j] -= dl;
        N[2 * j] -= dl; N[2 * j + 1] -= dl;
#pragma unroll
        for (int i = 0; i < 8; ++i) o[i][j] *= f;
      }
    }
    auto nxt = [&](unsigned s) { return s == P2 ? wrap : XB && s == P4 ? 0 : s + 32768; };
    prev = slot; slot = nxt(slot); kslot = nxt(kslot);
  };
  auto ring = [&](int g, int t, bool k, bool v) AI {
#pragma unroll
    for (int p = 0; p < 4; ++p) if (g == DG[p] && (p < 2 ? k : v)) piece(p, t, prev);
  };
#define KE 0
#define DHT {16, 17, 20, 21}
  constexpr int DH[4] = DHT;
  auto ring2 = [&](int g, int t, bool k0, bool k1, bool v1) AI {
    if (!KE) { ring(g, t, k0 || kh, true); return; }
    ring(g, t, k0 && !kh, !kh);
#pragma unroll
    for (int p = 0; p < 4; ++p) if (g == DH[p] && (p < 2 ? k1 : v1) && kh) piece(p, t + 1, slot);
  };
#define TOPBAR(W) do { if (!kh) { W; } } while (0)
#define WBN(n) do { if ((n) == 4) WAIT_BAR(4); else if ((n) == 5) WAIT_BAR(5); else if ((n) == 6) WAIT_BAR(6); else WAIT_BAR(8); } while (0)
#define WLATE 0
#define TW 8
  step(IC<1>{}, SA, SB, [&] { WAIT_BAR(S2L ? 4 : 8); }, [&](int g) AI { if (S2L == 1) ring(g, 2, true, true); if (KE && g >= 16) ring2(g, 2, true, true, true); });
  pace(); TOPBAR(BAR_ONLY());
  if (XB) wrap = P4;
  step(IC<1>{}, SB, SA, [&] { WAIT_BAR(XB ? 8 : 4); }, [&](int g) AI {
    if (XB) { if (g < 6) { const unsigned pv_ = prev; prev = P4; ring(g, 3, true, true); prev = pv_; } else ring(g - 6, 4, true, true); } else ring2(g, 3, true, true, true); });
  int nprev = XB ? 8 : 4;
#pragma unroll 1
  for (int t = 2; t < 28; t += 2) {
    const bool wc = t >= TW && t < TW + (CPF ? 18 : 16);
    const int ta = XB && t == 2 ? t + 1 : t;
    if (FB0 < 32 && t > FB0) adopt();
    if (t <= PEND) pace();
    TOPBAR(WBN(nprev));
    const bool ps_ = FB0 < 32 && t >= FB0 && t < 26 && late > fm;
    const int ncur = XB && t == 2 ? 8 : 4 + wc + ps_;
    step(IC<1>{}, SA, SB, [&] { WBN(ncur); }, [&](int g) AI { if (XB) ring(g, ta + 2, t != 4, t != 4); else ring2(g, t + 2, true, true, true);
      if (g == DG[3] + 2 && ps_) post();
      if (g == DG[3] + 1 && wc) { const int u = (t - TW) >> 1, i = CPF && u > 7 ? 7 : u;
        const unsigned dst = wdst + P3 + (i >> 1) * 16384 + (i & 1) * 1024;
        if (CPF && u == 7) {
          const unsigned l = lane_id();
          const char* a = CPF == 2 ? code_ahead(wid * 4096 + l * 64) : l < 32 ? wo + (wo_off(8) & ~255u) + (l >> 4) * 131072 + ((l >> 1) & 7) * 2048 + (l & 1) * 128 : code_ahead(wid * 4096 + (l - 32) * 128);
          asm volatile("s_mov_b32 m0, %1\n\ts_nop 0\n\tglobal_load_lds_dword %0, off" :: "v"(a), "s"(dst) : "memory");
        } else dma16<false>(wosrc, wo + wo_off(i), dst); } });
    wrap = 0;
    if (t < PEND) pace();
    TOPBAR(WBN(ncur));
    if (FB0 < 32) look_z();
    step(IC<1>{}, SB, SA, [&] { WBN(ncur); }, [&](int g) AI { if (XB) ring(g, ta + 3, true, true); else ring2(g, t + 3, true, true, true); });
    nprev = ncur;
  }
  MSTAMP(22);
  if (FB0 < 32) adopt();
  if (PEND > 27) pace();
  TOPBAR(WAIT_BAR(4));
  step(IC<1>{}, SA, SB, [&] { WAIT_BAR(WLATE ? 4 : 8); }, [&](int g) AI { ring2(g, 30, false, false, true); if (!WLATE && g >= 8 && g < 16 && !(g & 1)) ld_w((g - 8) >> 1); });
  MSTAMP(23);
  if (PEND > 28) pace();
  TOPBAR(WAIT_BAR(WLATE ? 2 : 6));
  step(IC<1>{}, SB, SA, [&] { WAIT_BAR(WLATE ? 2 : 10); }, [&](int g) AI { ring(g + DG[2], 31, false, !KE || !kh); if (!WLATE && g >= 8 && g < 16 && !(g & 1)) ld_w(4 + ((g - 8) >> 1)); });
  MSTAMP(24);
  if (PEND > 29) pace();
  TOPBAR(WAIT_BAR(WLATE ? 2 : 10));
  gu64* const fl1 = (gu64*)(ws + WS_F1) + (b * 4 + og) * 128, * const fl2 = (gu64*)(ws + WS_F2) + (b * 8 + oj) * 64, * const ep = (gu64*)(ws + WS_E) + b * 16;
  const unsigned ln = lane_id();
  const u64 ep0 = __hip_atomic_load(wid || ln < 16 ? ep : ln < 32 ? fl1 + (ln & 7) * 16 : fl2 + (ln & 3) * 16, RLX_AGENT);
  step(IC<0>{}, SA, SB, [&] { WAIT_BAR(WLATE ? 1 : 5); }, [&](int g) AI { if (g >= 16 && !(g & 1)) ld_w((WLATE ? 0 : 8) + ((g - 16) >> 1)); });
  MSTAMP(25);
  if (PEND > 30) pace();
  TOPBAR(WAIT_BAR(WLATE ? 9 : 13));

  const __amdgpu_buffer_rsrc_t wsr = __builtin_amdgcn_make_buffer_rsrc(ws, 0, WS_END, 0x00020000);
  const unsigned fo = wid * 4096 + ln * 16, obase = WS_O + (b * 4 + og) * (8 * 32768);
  u32x4 of[8][4];
  u64 fv;
#define W67 1
#define WT 1
#define OT 0
  unsigned tch = 0;
  if (WT) tch = __builtin_amdgcn_raw_buffer_load_b32(wor, ((ln >> 2) & 7) * 2048 + (ln & 3) * 64, wo_off(24 + 2 * (jj & 3)) & ~255u, 0);
  SBAR();
  {
    constexpr unsigned EL = XB ? P0 : P1, EA = XB ? P1 : P0;
    LAS f32x4* const ex = (LAS f32x4*)(lds + EA + kh * (P2 - EA) + qb * 8192 + ln * 16);
    const LAS f32x4* const pex = (const LAS f32x4*)(lds + EA + (kh ^ 1) * (P2 - EA) + qb * 8192 + ln * 16);
    LAS f32x4* const st = (LAS f32x4*)(lds + EL + wid * 1024 + ln * 16);
    const LAS f32x4* const pst = (const LAS f32x4*)(lds + EL + (wid ^ 4) * 1024 + ln * 16);
    constexpr int XL = LSM ? 21 : XG[15] + 2;
    float mx, f1[2], f2[2];
    f32x4 pv[8];
    f32x4 ps;
    auto factors = [&](int j) {
      const float mn = fmaxf(mref[j], ps[j]), a1 = __builtin_amdgcn_exp2f(mref[j] - mn), a2 = __builtin_amdgcn_exp2f(ps[j] - mn);
      const float inv = __builtin_amdgcn_rcpf(lsum[j] * a1 + ps[2 + j] * a2);
      f1[j] = a1 * inv; f2[j] = a2 * inv; PIN(f1[j]); PIN(f2[j]);
    };
    gaps(IC<0>{}, IC<XL>{}, IC<0>{}, IC<0>{}, SB, SA, mx, [&](int g) AI { if ((!W67 || WLATE) && g < 16 && (g & 1)) ld_w((WLATE ? 8 : 16) + (g >> 1)); });
    if (LSM) { lsum[0] = ls4[0][0]; lsum[1] = ls4[1][0]; }
    if (LSM < 2) { lsum[0] = rowsum4(lsum[0]); lsum[1] = rowsum4(lsum[1]); }
    *st = (f32x4){mref[0], mref[1], lsum[0], lsum[1]};
    SBAR();
    MSTAMP(26);
    if (kh) BAR_ONLY();
    MSTAMP(27);
    gaps(IC<XL>{}, IC<32>{}, IC<0>{}, IC<0>{}, SB, SA, mx, [&](int g) AI { const int p = g - 24;
      if (g == XL) ps = *pst;
      if (g == XL + 4 || g == XL + 5) factors(g - XL - 4);
      if (p >= 0) { ex[(2 * (p & 3) + (p >> 2)) * 64] = o[4 + (p & 3)][p >> 2]; pv[p] = pex[p * 64]; } });
    STAMP(4);
    if (!kh) { BAR_ONLY(); ps = *pst; factors(0); factors(1); }
    BAR_ONLY();
    if (!kh) {
#pragma unroll
      for (int i = 0; i < 8; ++i) pv[i] = pex[i * 64];
    }
#pragma unroll
    for (int i = 0; i < 8; ++i) pv[i] = pv[i] * f2[i & 1] + o[i >> 1][i & 1] * f1[i & 1];
    MSTAMP(13);
#pragma unroll
    for (int i = 0; i < 4; ++i) {
      const f32x4 u = pv[4 * (i >> 1) + (i & 1)], v = pv[4 * (i >> 1) + 2 + (i & 1)];
      of[0][i] = (u32x4){cvtpk(u[0], u[1]), cvtpk(u[2], u[3]), cvtpk(v[0], v[1]), cvtpk(v[2], v[3])};
      st16(of[0][i], wsr, fo + i * 1024, obase + pos * 32768);
    }
    MSTAMP(14);
    BAR_ONLY();
  }
#define LSH 0
#define NL 3
#define PD 6
#define PK0 4
#define PKF 3
#define XBAR 4
  constexpr int F0 = 16 * (8 - NL);
  constexpr auto fs = [](int F) { return F < F0 ? F >> 4 : 8 - NL + ((F - F0) >> 2) % NL; };
  constexpr auto fj = [](int F) { return F < F0 ? (F >> 3) & 1 : (F >> 1) & 1; };
  constexpr auto fn = [](int F) { return F < F0 ? F & 7 : 2 * ((F - F0) / (4 * NL)) + (F & 1); };
  f32x4 acc[8][2];
  bf16x8 af[8];
  const unsigned l4 = ln >> 4;
  const unsigned wosrc2 = ((ln >> 2) & 7) * 2048 + ((ln >> 5) ^ (~og & 1)) * 64 + ((ln & 3) ^ ((ln >> 3) & 2)) * 16;
  const lds_char* wa_ = lds + 16384 * kh + 2048 * (l4 >> 1) + 256 * (l4 & 1) + 64 * ((ln & 15) >> 2) + 8 * (ln & 3);
  const LdsBase wa0(wa_ + 32 * ((l4 ^ kh) & 1)), wa1(wa_ + 32 * ((l4 ^ kh ^ 1) & 1));
  const LdsBase ww(lds + wid * 2048 + ln * 16);
  constexpr unsigned WP[8] = {P3, P4, P0, P1, P2, P3, P4, P0};
  auto rd_h = [&](int F, int hi) { return lds_tr(((F & 1) ? wa1 : wa0).at(WP[fs(F)] + 8192 * fj(F) + 512 * (fn(F) >> 1) + 4096 * hi)); };
  auto wr_w = [&](int i) { *(LAS u32x4*)ww.at(WP[2 + (i >> 2)] + ((i >> 1) & 1) * 16384 + (i & 1) * 1024) = wl[i]; };
  auto frag = [&](int F) AI {
    s16x4 lo_;
#pragma unroll
    for (int qj = 0; qj < 2; ++qj) {
      acc[fn(F)][qj] = MFMA16(af[F & 7], asb(of[fs(F)][2 * fj(F) + qj]), F < 8 ? (f32x4)(0.f) : acc[fn(F)][qj]);
      SBAR();
      if (F + PD < 128) { if (qj == 0) lo_ = rd_h(F + PD, 0); else af[(F + PD) & 7] = cat8(lo_, rd_h(F + PD, 1)); }
      SBAR();
    }
  };
  u64 tag = (((u64)rfl((unsigned)(ep0 >> 32)) << 32) | rfl((unsigned)ep0)) + 1;
  asm volatile("" : "+s"(tag));
  int rdy = 0;
  auto look = [&](u64 v) { rdy = __builtin_ctz(~((unsigned)__builtin_amdgcn_ballot_w64(v == tag) & 255u | 1u << pos)); };
#define FW 1
  gu64* const pl1 = fl1 + (FW && (ln & 7) == pos ? pos ^ 1 : ln & 7) * 16;
  auto peek = [&] { if (wid == 0) fv = __hip_atomic_load(pl1, RLX_AGENT); };
  auto need = [&](int s) AI {
    const int r = rank_of(s);
    if (wid == 0 && rdy <= r) {
      look(fv);
#pragma unroll 1
      for (unsigned spins = 0; rdy <= r && spins < SPIN_CAP; ++spins) look(__hip_atomic_load(pl1, RLX_AGENT));
    }
  };
  auto ld_o = [&](int i) { of[i >> 2][i & 3] = __builtin_amdgcn_raw_buffer_load_b128(wsr, fo + (i & 3) * 1024, obase + rank_of(i >> 2) * 32768, 16); };
  SBAR();
#pragma unroll
  for (int i = 0; i < 12; ++i) { if (i == PK0) { peek(); SBAR(); } wr_w(i); SBAR(); }
#pragma unroll
  for (int F = 0; F < PD; ++F) af[F] = cat8(rd_h(F, 0), rd_h(F, 1));
  SBAR();
  MSTAMP(15);
  LAS f32x4* const ex = (LAS f32x4*)(lds + P1 + wid * 4096 + ln * 16);
  const LAS f32x4* const pex = (const LAS f32x4*)(lds + P1 + (wid ^ 4) * 4096 + ln * 16);
  const unsigned lin = wid * 1024 + ln * 16;
#define NX 0
#define SF0 1
#define SF1 3
  const unsigned pbase = WS_P + (b * 8 + oj) * (131072 << NX);
  auto send2 = [&](int t, int e) {
    const f32x4 r0 = acc[2 * t + e][0], r1 = acc[2 * t + e][1];
    st16((u32x4){cvtpk(r0[0], r0[1]), cvtpk(r0[2], r0[3]), cvtpk(r1[0], r1[1]), cvtpk(r1[2], r1[3])}, wsr, ln * 16, pbase + ((t ^ og ^ 3) * 4 + og) * 16384 + e * 8192 + (wid ^ 4 * e) * 1024);
  };
  f32x4 pv[2];
  auto send = [&](int t) {
    const f32x4 r0 = acc[2 * t][0] + pv[0], r1 = acc[2 * t][1] + pv[1];
    st16((u32x4){cvtpk(r0[0], r0[1]), cvtpk(r0[2], r0[3]), cvtpk(r1[0], r1[1]), cvtpk(r1[2], r1[3])}, wsr, lin, pbase + ((t ^ og ^ 3) * 4 + og) * 8192);
  };
#define OPT {40, 56, 16, 24, 32, 48, 64, 72}
#define OPD 2
  constexpr auto opf = [](int n) { constexpr int T[8] = OPT; return T[n >> 2] + OPD * (n & 3); };
  constexpr auto younger_ops = [=](int f0, int f1) { int c = 0; for (int n = 0; n < 32; ++n) c += opf(n) > f0 && opf(n) <= f1; return c; };
#define BRT {0, 16, 32, 48, 64, 80}
#define WF5 16
#define VW6 64
#define VW7 80
#define PKB 13
  constexpr auto nextbar = [](int f) { constexpr int T[] = BRT; for (int x : T) if (x > f) return x; return 128; };
  constexpr auto lastbar = [](int f) { constexpr int T[] = BRT; int r = 0; for (int x : T) if (x <= f) r = x; return r; };
  constexpr auto needs = [=](int f) { int s = f == 0; for (int g = 2; g < 8; ++g) if (opf(4 * g) >= f && opf(4 * g) < nextbar(f)) s = g; return s; };
  sfor<0, 128>([&](auto fc) AI {
    constexpr int F = fc.value, t = F < F0 ? -1 : (F - F0) / (4 * NL) - 1, ph = F < F0 ? -1 : (F - F0) % (4 * NL);
    if constexpr (F == 0) {
      WAIT_BAR(0);
      STAMP(5);
      if (WT) use(tch);
      if (OT) tch = __builtin_amdgcn_raw_buffer_load_b32(wsr, wid * 4096 + ln * 64, obase + pos * 32768, 16);
      if (wid == 0) look(fv);
      if (wid == FW && ln == 0) __hip_atomic_store(fl1 + pos * 16, tag, RLX_AGENT);
    }
    if constexpr (lastbar(F) == F) { if constexpr (needs(F) > 0) need(needs(F)); }
    if (F == 0) STAMP(6);
    if (F == 16) STAMP(7);
    if (F == 20) STAMP(8);
    if (lastbar(F) == F) asm volatile("s_barrier" ::: "memory");
    {
      constexpr auto pr = [=](int f) { const int x0 = F0 + 4 * NL + XBAR, l = lastbar(f), n = nextbar(f) < 128 || NX ? nextbar(f) : x0;
        return f < n ? 3 - 4 * (f - l) / (n - l) : 3 - ((f - x0) % (4 * NL)) / NL; };
      if constexpr (F == 0 || pr(F) != pr(F - 1)) __builtin_amdgcn_s_setprio(pr(F));
    }
    if constexpr (F == 0) {
#pragma unroll
      for (int i = 4; i < 8; ++i) ld_o(i);
      SBAR();
    }
    if constexpr (F == WF5) {
      if (OT) use(tch);
#pragma unroll
      for (int i = 12; i < 16; ++i) wr_w(i);
      SBAR();
    }
    if (!NX && t >= 0 && ph == XBAR) {
      constexpr int younger = 2 * ((F + PD < 128 ? F + PD : 128) - (F - XBAR + 2 + PD < 128 ? F - XBAR + 2 + PD : 128));
      asm volatile("s_waitcnt lgkmcnt(%0)\n\ts_barrier" :: "i"(younger) : "memory");
      pv[0] = pex[(t & 1) * 128]; pv[1] = pex[(t & 1) * 128 + 64];
    }
    frag(F);
    if constexpr (F + PKB == nextbar(F) && F + PKB < 128) { if constexpr (needs(F + PKB) > 0) peek(); }
    sfor<0, 32>([&](auto nc) AI { constexpr int g = nc.value >> 2, i = 4 * (g & 7) + (nc.value & 3);
      if constexpr (opf(nc.value) == fc.value) {
        if constexpr (g >= 2) ld_o(i);
        else dma16<false>(wosrc2, wo + wo_off(24 + i), wdst + WP[6 + g] + ((i >> 1) & 1) * 16384 + (i & 1) * 1024); } });
    if constexpr (F == VW6 - 1) asm volatile("s_waitcnt vmcnt(%0)" :: "i"(younger_ops(opf(3), F)) : "memory");
    if constexpr (F == VW7 - 1) asm volatile("s_waitcnt vmcnt(%0)" :: "i"(younger_ops(opf(7), F)) : "memory");
    if constexpr (t >= 0 && NX) { if (ph == SF0) send2(t, 0); if (ph == SF1) send2(t, 1); }
    if constexpr (t >= 0 && !NX) {
      if (ph == 1) { ex[(t & 1) * 128] = acc[2 * t + 1][0]; ex[(t & 1) * 128 + 64] = acc[2 * t + 1][1]; }
      if (ph == XBAR + 3) send(t);
    }
    SBAR();
  });
  STAMP(9);
  {
    ex[128] = acc[7][0]; ex[192] = acc[7][1];
    WAIT_BAR(0);
    STAMP(10);
#define FW2 FW
    if (wid == FW2 && ln == 0) __hip_atomic_store(fl2 + og * 16, tag, RLX_AGENT);
    if (wid == 0) {
#pragma unroll 1
      for (unsigned spins = 0; spins < SPIN_CAP; ++spins)
        if (((unsigned)__builtin_amdgcn_ballot_w64(__hip_atomic_load(fl2 + (FW2 && (ln & 3) == og ? og ^ 1 : ln & 3) * 16, RLX_AGENT) == tag) & 15u | 1u << og) == 15u) break;
      if (h == 0 && ln == 0) __hip_atomic_store(ep, tag, RLX_AGENT);
    }
    f32x4 v[2] = {acc[6][0] + pex[128], acc[6][1] + pex[192]};
    BAR_ONLY();
    STAMP(11);
    u32x4 pl[3 << NX];
#pragma unroll
    for (int i = 0; i < 3 << NX; ++i) pl[i] = __builtin_amdgcn_raw_buffer_load_b128(wsr, lin, pbase + ((og * 4 + ((og + 1 + (i >> NX)) & 3) << NX) + (i & NX)) * 8192, 16);
#pragma unroll
    for (int i = 0; i < 3 << NX; ++i)
#pragma unroll
      for (int e = 0; e < 2; ++e)
        v[e] += (f32x4){__uint_as_float(pl[i][2 * e] << 16), __uint_as_float(pl[i][2 * e] & 0xffff0000u),
                        __uint_as_float(pl[i][2 * e + 1] << 16), __uint_as_float(pl[i][2 * e + 1] & 0xffff0000u)};
    asm volatile(".Lmha_end:");
    auto s0 = __builtin_amdgcn_permlane16_swap(cvtpk(v[0][0], v[0][1]), cvtpk(v[1][0], v[1][1]), false, false);
    auto s1 = __builtin_amdgcn_permlane16_swap(cvtpk(v[0][2], v[0][3]), cvtpk(v[1][2], v[1][3]), false, false);
    *(u32x4*)(out + ((size_t)(b * SQ + 32 * qb + (ln & 31)) * DM + 128 * oj + 32 * og + 16 * kh + 8 * (ln >> 5)) * 2) = (u32x4){s0[0], s1[0], s0[1], s1[1]};
  }
}

extern "C" void kernel_launch(void* const* d_in, const int* in_sizes, int n_in,
                              void* d_out, int out_size, void* d_ws, size_t ws_size,
                              hipStream_t stream) {
  (void)hipFuncSetAttribute((const void*)mha_kernel, hipFuncAttributeMaxDynamicSharedMemorySize, LDS1);
  mha_kernel<<<256, 512, LDS1, stream>>>((const char*)d_in[0], (const char*)d_in[1], (const char*)d_in[2], (const char*)d_in[3],
                                         (const char*)d_in[4], (char*)d_ws, (char*)d_out);
}
```

```cpp
#include <hip/hip_runtime.h>
#include <hip/hip_bf16.h>
#include <stdint.h>

typedef float    f32x4  __attribute__((ext_vector_type(4)));
typedef float    f32x2  __attribute__((ext_vector_type(2)));
typedef __bf16   bf16x8 __attribute__((ext_vector_type(8)));
typedef __bf16   bf16x2 __attribute__((ext_vector_type(2)));
typedef short    s16x4  __attribute__((ext_vector_type(4)));
typedef short    s16x8  __attribute__((ext_vector_type(8)));
typedef unsigned u32x4  __attribute__((ext_vector_type(4)));
typedef unsigned u32x2  __attribute__((ext_vector_type(2)));
typedef unsigned long long u64;
#define LAS __attribute__((address_space(3)))
typedef LAS char lds_char;

#define DI __device__ __forceinline__
#define AI __attribute__((always_inline))
#define SBAR() __builtin_amdgcn_sched_barrier(0)
#define PIN(x) asm volatile("" : "+v"(x))

constexpr int SQ = 128, DM = 1024, NH = 32, SK = 2048, DH = 128;
constexpr int LDS1 = 163840;
constexpr float QSCALE = 0.08838834764831845f * 1.4426950408889634f;
#define RTHR 8.0f
constexpr unsigned P0 = 0, P1 = 32768, P2 = 65536, P3 = 98304, P4 = 131072;

constexpr unsigned WS_O = 0, WS_P = 8u << 20, WS_F1 = 24u << 20, WS_F2 = WS_F1 + 32768, WS_E = WS_F2 + 32768, WS_END = WS_E + 2048;
constexpr unsigned WS_L = 25u << 20, FST = 4352;
constexpr unsigned SPIN_CAP = 1u << 17;
typedef __attribute__((address_space(1))) u64 gu64;
#define RLX_AGENT __ATOMIC_RELAXED, __HIP_MEMORY_SCOPE_AGENT

DI unsigned rfl(unsigned x) { return __builtin_amdgcn_readfirstlane(x); }
DI unsigned cvtpk(float lo, float hi) { f32x2 v = {lo, hi}; bf16x2 b = __builtin_convertvector(v, bf16x2); return __builtin_bit_cast(unsigned, b); }
DI float max3f(float a, float b, float c) { return __builtin_fmaxf(__builtin_fmaxf(a, b), c); }
DI float rowmax4(float v) {
  auto a = __builtin_amdgcn_permlane16_swap(__float_as_uint(v), __float_as_uint(v), false, false);
  v = fmaxf(__uint_as_float(a[0]), __uint_as_float(a[1]));
  auto c = __builtin_amdgcn_permlane32_swap(__float_as_uint(v), __float_as_uint(v), false, false);
  return fmaxf(__uint_as_float(c[0]), __uint_as_float(c[1]));
}
DI float rowsum4(float v) {
  auto a = __builtin_amdgcn_permlane16_swap(__float_as_uint(v), __float_as_uint(v), false, false);
  v = __uint_as_float(a[0]) + __uint_as_float(a[1]);
  auto c = __builtin_amdgcn_permlane32_swap(__float_as_uint(v), __float_as_uint(v), false, false);
  return __uint_as_float(c[0]) + __uint_as_float(c[1]);
}
DI float max8(f32x4 a, f32x4 b) { return fmaxf(max3f(max3f(a[0], a[1], a[2]), a[3], b[0]), max3f(b[1], b[2], b[3])); }

template <bool NT> DI void dma16(unsigned voff, const void* sbase, unsigned ldsdst) {
  if constexpr (NT) asm volatile("s_mov_b32 m0, %2\n\ts_nop 0\n\tglobal_load_lds_dwordx4 %0, %1 nt" :: "v"(voff), "s"(sbase), "s"(ldsdst) : "memory");
  else              asm volatile("s_mov_b32 m0, %2\n\ts_nop 0\n\tglobal_load_lds_dwordx4 %0, %1" :: "v"(voff), "s"(sbase), "s"(ldsdst) : "memory");
}
DI void dma16_if(int a, int b, unsigned voff, const void* sbase, unsigned ldsdst) {
  asm volatile("s_mov_b32 m0, %2\n\ts_cmp_lg_u32 %3, %4\n\ts_cbranch_scc0 .Lmha_nd%=\n\tglobal_load_lds_dwordx4 %0, %1 nt\n.Lmha_nd%=:"
               :: "v"(voff), "s"(sbase), "s"(ldsdst), "s"(a), "s"(b) : "memory", "scc");
}
DI void post_time(int c, unsigned voff, const void* sbase) {
  u64 t, v;
  asm volatile("s_cmp_lg_u32 %4, 0\n\ts_cbranch_scc1 .Lmha_pt%=\n\ts_memrealtime %0\n\ts_waitcnt lgkmcnt(0)\n\tv_mov_b64 %1, %0\n\t"
               "global_store_dwordx2 %2, %1, %3 sc1\n\ts_nop 1\n.Lmha_pt%=:" : "=&s"(t), "=&v"(v) : "v"(voff), "s"(sbase), "s"(c) : "memory", "scc");
}
DI void st16(u32x4 v, __amdgpu_buffer_rsrc_t r, unsigned voff, unsigned soff) {
  __builtin_amdgcn_raw_buffer_store_b128(v, r, voff, soff, 16);
  asm volatile("s_nop 1" :: "v"(v));
}
#define CODE_AT(label, a, len) asm volatile("s_getpc_b64 %0\n.Lmha_pc%=:\n\ts_mov_b32 %1, " label "-.Lmha_pc%=\n\ts_mov_b32 %2, .Lmha_end-" label \
                                            : "=s"(a), "=s"(len##_), "=s"(len)); a = (const char*)((uintptr_t)(a + len##_) & ~(uintptr_t)63); len &= ~63u
template <int V> struct IC { static constexpr int value = V; };
template <int I, int N, class F> DI void sfor(F f) { if constexpr (I < N) { f(IC<I>{}); sfor<I + 1, N>(f); } }
DI void use(unsigned v) { asm volatile("" :: "v"(v)); }
DI unsigned lane_id() { unsigned l; asm volatile("v_mbcnt_lo_u32_b32 %0, -1, 0\n\tv_mbcnt_hi_u32_b32 %0, -1, %0" : "=v"(l)); return l; }
DI unsigned now10() { return (unsigned)__builtin_amdgcn_s_memrealtime(); }
struct LdsBase {
  const lds_char* p[3];
  DI LdsBase(const lds_char* q) { p[0] = q; p[1] = q + 65536; p[2] = q + 131072; PIN(p[1]); PIN(p[2]); }
  DI const lds_char* at(unsigned off) const { return p[off >> 16] + (off & 65535); }
};
DI bf16x8 lds_rd128(const lds_char* p) { return *(const LAS bf16x8*)p; }
DI s16x4 lds_tr(const lds_char* p) { return __builtin_amdgcn_ds_read_tr16_b64_v4i16((LAS s16x4*)p); }
DI bf16x8 cat8(s16x4 a, s16x4 b) { s16x8 r = __builtin_shufflevector(a, b, 0, 1, 2, 3, 4, 5, 6, 7); return __builtin_bit_cast(bf16x8, r); }
DI bf16x8 asb(u32x4 v) { return __builtin_bit_cast(bf16x8, v); }
#define MFMA16(a, b, c) __builtin_amdgcn_mfma_f32_16x16x32_bf16(a, b, c, 0, 0, 0)
#define WAIT_BAR(N) asm volatile("s_waitcnt vmcnt(%0) lgkmcnt(0)\n\ts_barrier" :: "i"(N) : "memory")
#define BAR_ONLY() asm volatile("s_waitcnt lgkmcnt(0)\n\ts_barrier" ::: "memory")
#define WAIT_VM0() asm volatile("s_waitcnt vmcnt(0)" ::: "memory")

#define KNT true
#define VNT true
#define QRG 14

__global__ __launch_bounds__(512, 2)
void mha_kernel(const char* __restrict__ x, const char* __restrict__ kc, const char* __restrict__ vc,
                const char* __restrict__ wq, const char* __restrict__ wo, char* ws, char* __restrict__ out) {
  extern __shared__ __attribute__((aligned(16))) char smem[];
  lds_char* lds = (lds_char*)smem;
  const unsigned lds0 = (unsigned)(uintptr_t)lds;
  const int tid = threadIdx.x, lane = tid & 63;
  const int wid = rfl(tid >> 6);
  const int bid = blockIdx.x;
  const int xg = bid & 7, jj = bid >> 3;
  const int og = xg >> 1, oj = jj >> 2;
  const int b = (xg & 1) * 4 + (jj & 3), h = og * 8 + oj;
  const int bh = b * NH + h;
  const int pos = (0x53724160 >> 4 * oj) & 7;
  auto head_of = [](int r) { return (0x51736420 >> 4 * r) & 7; };
  const unsigned tstart = now10();

  const int qb = wid & 3, kh = wid >> 2;
  const unsigned dr = (lane >> 2) & 7, dblk = lane >> 5, dch = lane & 3;
#define KPM 0x496a7352810ull
  constexpr auto pm = [](unsigned v, int j0, int n) { unsigned r = 0; for (int j = 0; j < n; ++j) r |= ((v >> j) & 1) << (8 + ((KPM >> 4 * (j0 + j)) & 15)); return r; };
  constexpr int R32 = pm(32, 0, 6);
  const unsigned ksrc = pm(8 * wid + dr, 0, 6) + dblk * 64 + ((dch ^ (2 * (wid & 1))) * 16);
  const unsigned vsrc = pm(8 * wid + dr, 0, 6) + dblk * 64 + ((dch ^ (2 * (dr >> 2))) * 16);
  const char* kbase = kc + (size_t)bh * (SK * DH * 2);
  const char* vbase = vc + (size_t)bh * (SK * DH * 2);
  const unsigned wdst = lds0 + wid * 2048;
  const int trot = (bh >> 3 & 6) | (bh << 1 & 16);
  auto tile = [&](int t) { return (int)pm(t ^ trot, 6, 5); };
  const unsigned vd0 = wdst + 16384 + kh * 1024, vd1 = wdst + 16384 + 1024 - kh * 1024;

  const unsigned g4 = lane >> 4, r16 = lane & 15, vq = r16 >> 2;
  constexpr unsigned QP[3] = {P2, P1, P4};
#define X4 1
#define KV0 1
#define KV1 4
#define KV2 7
#define KV3 10
  if (kh) {
#define QA 8
#define QB 8
#define QC 2
#define QD 1
    const int qrot = (QA * (jj & 3) + QB * (jj >> 2) + QC * (xg >> 1) + QD * (xg & 1)) & 15;
    const char* xb = x + (size_t)b * (SQ * DM * 2) + qb * 65536;
    const char* wb = wq + h * 256 + qb * 131072;
    const unsigned xsrc0 = dr * 2048 + dblk * 64 + dch * 16, xsrc1 = (8 + dr) * 2048 + dblk * 64 + (dch ^ 2) * 16;
    const unsigned wsrc[2] = {dr * 8192 + dblk * 64 + dch * 16, dr * 8192 + dblk * 64 + (dch ^ 2) * 16};
    const unsigned ld = lds0 + qb * 4096;
    auto issue_q = [&](int i, unsigned reg) {
      const int blk = (i + qrot) & 15;
      const char* xs = xb + blk * 128; const char* wsq = wb + (size_t)blk * (64 * 8192);
#pragma unroll
      for (int p = 0; p < 2; ++p) {
        dma16<false>(xsrc0, xs + p * 32768, ld + reg + p * 2048);
        dma16<false>(xsrc1, xs + p * 32768, ld + reg + p * 2048 + 1024);
        dma16<false>(wsrc[p], wsq + p * 65536, ld + reg + 16384 + p * 2048);
        dma16<false>(wsrc[p], wsq + p * 65536 + 128, ld + reg + 16384 + p * 2048 + 1024);
      }
    };
    auto pf_k = [&](int t, unsigned reg, int p) {
      dma16<KNT>(ksrc, kbase + (p < 2 ? tile(t) - R32 : tile(t >= 2 ? t - 1 : t)) + 128 * (p & 1), wdst + reg - (p < 2 ? 8192 : 0) + 1024 * (p & 1)); };
    auto pf_v = [&](int t, unsigned reg, int p) {
      dma16<VNT>(vsrc, vbase + tile(t) - (p < 2 ? R32 : 0) + 128 * (p & 1), p < 2 ? wdst + reg + 8192 + 1024 * p : (p & 1 ? vd1 : vd0) + reg); };
#define KVT {KV0, KV0, KV0, KV0, KV1, KV1, KV1, KV1, KV2, KV2, KV2, KV2, KV3, KV3, KV3, KV3, 13, 13, 13, 13, 13, 13, 13, 13}
    constexpr int KVI[24] = KVT;
    auto kvissue = [&](int it) {
#pragma unroll
      for (int j = 0; j < (X4 ? 32 : 24); ++j) if (it == (j < 24 ? KVI[j] : 15)) {
        const int u = j >> 2;
        if (u == 2 || u == 5 || u == 7) pf_v(u / 2 - 1, u == 2 ? P0 : u == 5 ? P1 : P2, j & 3);
        else pf_k(u - (u > 2) - (u > 5), u == 0 ? P3 : u == 1 ? P3 + 16384 : u == 3 ? P0 : u == 4 ? P1 : P2, j & 3);
      }
    };
#define QT 6
    constexpr auto kvn = [](int it) { constexpr int T[24] = KVT; int n = it == QT; for (int j = 0; j < 24; ++j) n += T[j] == it; return n; };
    issue_q(0, QP[0]);
    issue_q(1, QP[1]); issue_q(2, QP[2]);
    WAIT_BAR(16);
    sfor<0, 16>([&](auto ic) AI {
      constexpr int i = ic.value;
#define VM_BAR(N) asm volatile("s_waitcnt vmcnt(%0)\n\ts_barrier" :: "i"(N) : "memory")
      VM_BAR(i == 15 ? 63 : kvn(i - 2) + (i + 2 < 16 ? 8 : 0) + kvn(i - 1));
      if (i + 3 < 16) issue_q(i + 3, QP[i % 3]);
      kvissue(i);
      if constexpr (i == QT) {
        const char* a; unsigned n_, n; CODE_AT(".Lmha_att", a, n);
        dma16<false>(min((jj & 3) * 4096 + qb * 1024 + (lane >> 2) * 64u, n), a, wdst + P0);
      }
    });
    WAIT_BAR(X4 ? 16 : 8);
  } else {
    const int qh = wid & 1, dh = wid >> 1;
    f32x4 qa[16];
#pragma unroll
    for (int i = 0; i < 16; ++i) qa[i] = (f32x4)(0.f);
    const LdsBase xr(lds + (8 * qh + (r16 >> 3)) * 1024 + 64 * (r16 & 7) + 16 * (g4 ^ ((r16 >> 3) * 2)));
    const LdsBase wr0(lds + 1024 * dh + 2048 * g4 + 64 * vq + 32 * (g4 & 1) + 8 * (lane & 3));
    const LdsBase wr1(lds + 1024 * dh + 2048 * g4 + 64 * vq + 32 * ((g4 & 1) ^ 1) + 8 * (lane & 3));
    auto rd_x = [&](unsigned reg, int g) { return lds_rd128(xr.at(reg + 512 * (g >> 2) + 2048 * (g & 3))); };
    auto rd_wq = [&](unsigned reg, int g) { const unsigned o_ = reg + 16384 + 8192 * (g >> 2) + 512 * ((g >> 1) & 1); const LdsBase& w_ = (g & 1) ? wr1 : wr0;
                                            return cat8(lds_tr(w_.at(o_)), lds_tr(w_.at(o_ + 256))); };
    BAR_ONLY();
    bf16x8 xf[2][8], wf[2][8];
#pragma unroll
    for (int g = 0; g < 8; ++g) { xf[0][g] = rd_x(QP[0], g); wf[0][g] = rd_wq(QP[0], g); }
    sfor<0, 16>([&](auto ic) AI {
      constexpr int i = ic.value, c = i & 1, n = c ^ 1;
      constexpr unsigned reg = QP[(i + 1) % 3];
      BAR_ONLY();
#define PI 12
      if constexpr (i == PI) post_time(wid | jj & 15, (lane & 7) * (4 * FST), ws + WS_L + (xg & 3) * FST + (xg & 4) * 4 + (jj >> 4) * 8);
      s16x4 lo_;
#pragma unroll
      for (int g = 0; g < 32; ++g) {
        const int dj = (g >> 2) & 3, qj = g & 3;
        qa[4 * dj + qj] = MFMA16(wf[c][4 * (g >> 4) + dj], xf[c][4 * (g >> 4) + qj], qa[4 * dj + qj]);
#define SLT 0x4444ull
        if constexpr ((SLT >> 4 * i) & 15) asm volatile("s_nop %0" :: "i"(((SLT >> 4 * i) & 15) - 1));
        SBAR();
        if (i < 15) {
#pragma unroll
          for (int e = 0; e < 24; ++e) if (e * QRG / 12 == g) {
            const int f = e / 3;
            const unsigned o_ = reg + 16384 + 8192 * (f >> 2) + 512 * ((f >> 1) & 1); const LdsBase& w_ = (f & 1) ? wr1 : wr0;
            if (e % 3 == 0) xf[n][f] = rd_x(reg, f);
            if (e % 3 == 1) lo_ = lds_tr(w_.at(o_));
            if (e % 3 == 2) wf[n][f] = cat8(lo_, lds_tr(w_.at(o_ + 256)));
          }
        }
        SBAR();
      }
    });
    asm volatile(".Lmha_att:");
    lds_char* qw = lds + P4 + 16384 * qh + 2048 * dh + 256 * (g4 >> 1) + 16 * r16 + 8 * (g4 & 1);
#pragma unroll
    for (int t = 0; t < 16; ++t) {
      u32x2 w = {cvtpk(qa[t][0] * QSCALE, qa[t][1] * QSCALE), cvtpk(qa[t][2] * QSCALE, qa[t][3] * QSCALE)};
      *(LAS u32x2*)(qw + 4096 * (t & 3) + 512 * (t >> 2)) = w;
    }
    BAR_ONLY();
  }
  u64 post;
  if (PI < 16) asm volatile("s_cmp_lg_u32 %3, 0\n\ts_cbranch_scc1 .Lmha_np%=\n\tglobal_load_dwordx2 %0, %1, %2 sc1\n.Lmha_np%=:"
                            : "=v"(post) : "v"((r16 & 3) * FST + (r16 & 12) * 2), "s"(ws + WS_L + xg * (4 * FST)), "s"(wid) : "scc");
  const unsigned rrow = 2048 * (r16 >> 3) + 64 * (r16 & 7) + 16 * (g4 ^ (2 * (r16 >> 3)));
  const lds_char* kr = lds + 8192 * kh + rrow;
  const lds_char* vr0 = lds + 16384 + 8192 * kh + 2048 * (g4 >> 1) + 256 * (g4 & 1) + 64 * vq + 8 * (lane & 3) + 32 * (g4 & 1);
  const lds_char* vr1 = lds + 16384 + 8192 * kh + 2048 * (g4 >> 1) + 256 * (g4 & 1) + 64 * vq + 8 * (lane & 3) + 32 * ((g4 & 1) ^ 1);
  bf16x8 qf[8], kf[8];
  f32x4 o[8][2], SA[4], SB[4], negm[2];
  float mref[2], lsum[2] = {0.f, 0.f};
#define LSM 1
  f32x4 ls4[2] = {(f32x4)(0.f), (f32x4)(0.f)};
  u32x4 ones = (u32x4)(LSM == 2 || r16 == 0 ? 0x3f803f80u : 0u); PIN(ones);
#pragma unroll
  for (int i = 0; i < 8; ++i) { qf[i] = lds_rd128(lds + P4 + 8192 * qb + 16 * lane + 4096 * (i & 1) + 1024 * (i >> 1)); kf[i] = lds_rd128(kr + P3 + 4096 * (i & 1) + 512 * (i >> 1)); }
#pragma unroll
  for (int i = 0; i < 8; ++i) { o[i][0] = (f32x4)(0.f); o[i][1] = (f32x4)(0.f); }
#pragma unroll
  for (int g = 0; g < 16; ++g) SA[2 * (g & 1) + ((g >> 1) & 1)] = MFMA16(kf[g >> 1], qf[2 * (g >> 2) + (g & 1)], g < 4 ? (f32x4)(0.f) : SA[2 * (g & 1) + ((g >> 1) & 1)]);
  SBAR();
  if (X4 && kh) { asm volatile("s_barrier" ::: "memory"); SBAR(); }
#pragma unroll
  for (int i = 0; i < 8; ++i) kf[i] = lds_rd128(kr + P3 + 16384 + 4096 * (i & 1) + 512 * (i >> 1));
  int ttab = tile(lane_id() & 31); PIN(ttab);
  auto tl = [&](int t) { return __builtin_amdgcn_readlane(ttab, t); };
  auto piece = [&](int p, int t, unsigned reg) AI {
    if (p < 2) dma16<KNT>(ksrc, kbase + tl(t + 2 - kh) + 128 * p, wdst + reg + 1024 * p);
    else dma16<VNT>(vsrc, vbase + tl(t) + 128 * (p & 1), (p & 1 ? vd1 : vd0) + reg);
  };
#pragma unroll
  for (int j = 0; j < 2; ++j) {
    mref[j] = rowmax4(max8(SA[2 * j], SA[2 * j + 1]));
    SA[2 * j] -= mref[j]; SA[2 * j + 1] -= mref[j];
    negm[j] = (f32x4)(-mref[j]); PIN(negm[j]);
  }

  auto rank_of = [&](int s) { return s == 0 ? pos : s - 1 + (s - 1 >= pos); };
  const unsigned wosrc = dr * 2048 + (dblk ^ (~og & 1)) * 64 + (dch ^ (2 * (dr >> 2))) * 16;
  auto wo_off = [&](int i) {
    return (unsigned)((og * 8 + head_of(rank_of(i >> 2))) * 128 + ((i >> 1) & 1) * 64 + wid * 8) * 2048 + oj * 256 + (((i & 1) ^ (~og >> 1)) & 1) * 128; };
  const __amdgpu_buffer_rsrc_t wor = __builtin_amdgcn_make_buffer_rsrc((void*)wo, 0, NH * DH * DM * 2, 0x00020000);
  u32x4 wl[16];
  auto ld_w = [&](int i) { wl[i] = __builtin_amdgcn_raw_buffer_load_b128(wor, wosrc, wo_off(8 + i), 0); };

#define PACE 113
#define PT0 1000
#define SPR PACE
#define PEND 29
#define PHT 0x10765432
  unsigned pdl = tstart + PT0 + (SPR * (((PHT >> 4 * pos) & 15) * 32 + (jj & 3) * 8 + xg) >> 8);
  unsigned per = PACE;
  auto pace = [&] { if (wid == 0) { pdl += per; while ((int)(now10() - pdl) < 0) __builtin_amdgcn_s_sleep(1); } };

#define VRN 16
#define XGT {0, 1, 2, 3, 4, 5, 6, 7, 9, 10, 11, 12, 13, 14, 15, 16}
#define KGT {16, 18, 20, 22, 24, 26, 28, 30}
#define MGT {19, 20, 21, 22, 23, 24, 25, 26}
#define DGT {0, 1, 4, 5}
  constexpr int XG[16] = XGT, KG[8] = KGT, MG[8] = MGT;
  constexpr int DG[4] = DGT;
  constexpr auto pvd = [](int p) { return ((p & 8) ? 0 : 4) + (p & 3); };
  constexpr auto pvq = [](int p) { return (p >> 2) & 1; };
  unsigned slot = 0, kslot = kh * 32768, prev = X4 ? P4 : P2, wrap = X4 ? P4 : 0;
  bf16x8 vf[8]; u32x4 pw[2];
  auto gaps = [&](auto g0, auto g1, auto qk, auto kread, f32x4 (&C)[4], f32x4 (&N)[4], float& mx, auto extra) AI {
    const lds_char* v0_ = vr0 + slot; const lds_char* v1_ = vr1 + slot; const lds_char* k_ = kr + kslot;
    s16x4 lo_;
#pragma unroll
    for (int g = g0.value; g < g1.value; ++g) {
      if (g < 16) {
        const int qi = g & 1, si = 2 * qi + ((g >> 1) & 1);
        if (qk.value) N[si] = MFMA16(kf[g >> 1], qf[2 * (g >> 2) + qi], g < 4 ? negm[qi] : N[si]);
        SBAR();
#pragma unroll
        for (int r = 0; r < 16; ++r) if (r * VRN / 16 == g) {
          const int db = (r >> 1) ^ 4;
          const lds_char* a_ = ((db & 1) ? v1_ : v0_) + 512 * (db >> 1) + 4096 * (r & 1);
          if (r & 1) vf[db] = cat8(lo_, lds_tr(a_)); else lo_ = lds_tr(a_);
        }
      } else {
        if (LSM && (g == 16 || g == 20)) { ls4[pvq(g - 16)] = MFMA16(asb(ones), asb(pw[pvq(g - 16)]), ls4[pvq(g - 16)]); SBAR(); }
        o[pvd(g - 16)][pvq(g - 16)] = MFMA16(vf[pvd(g - 16)], asb(pw[pvq(g - 16)]), o[pvd(g - 16)][pvq(g - 16)]);
        SBAR();
      }
#pragma unroll
      for (int e = 0; e < 16; ++e) if (XG[e] + 1 == g) {
        if (!LSM) { lsum[e >> 3] += C[e >> 2][e & 3]; PIN(lsum[e >> 3]); }
        if (e & 1) { pw[e >> 3][(e >> 1) & 3] = cvtpk(C[e >> 2][e & 2], C[e >> 2][e & 3]); PIN(pw[e >> 3]); } }
#pragma unroll
      for (int e = 0; e < 16; ++e) if (XG[e] == g) { C[e >> 2][e & 3] = __builtin_amdgcn_exp2f(C[e >> 2][e & 3]); PIN(C[e >> 2]); }
#pragma unroll
      for (int i = 0; i < 8; ++i) {
        if (kread.value && KG[i] == g) kf[i] = lds_rd128(k_ + 4096 * (i & 1) + 512 * (i >> 1));
        if (qk.value && MG[i] == g) { mx = max3f(mx, N[i >> 1][2 * (i & 1)], N[i >> 1][2 * (i & 1) + 1]); PIN(mx); } }
      extra(g);
      SBAR();
    }
  };
  auto step = [&](auto kread, f32x4 (&C)[4], f32x4 (&N)[4], auto mid, auto extra) AI {
    float mx = RTHR; PIN(mx);
    gaps(IC<0>{}, IC<16>{}, IC<1>{}, kread, C, N, mx, extra);
    if (kh) mid();
    gaps(IC<16>{}, IC<32>{}, IC<1>{}, kread, C, N, mx, extra);
    if (__builtin_expect(__any(mx > RTHR), 0)) {
#pragma unroll
      for (int j = 0; j < 2; ++j) {
        const float dl = fmaxf(rowmax4(max8(N[2 * j], N[2 * j + 1])), 0.f), f = __builtin_amdgcn_exp2f(-dl);
        mref[j] += dl; lsum[j] *= f; ls4[j][0] *= f; negm[j] -= dl;
        N[2 * j] -= dl; N[2 * j + 1] -= dl;
#pragma unroll
        for (int i = 0; i < 8; ++i) o[i][j] *= f;
      }
    }
    auto nxt = [&](unsigned s) { return s == P2 ? wrap : X4 && s == P4 ? 0 : s + 32768; };
    prev = slot; slot = nxt(slot); kslot = nxt(kslot);
  };
  auto ring = [&](int g, int t, bool k, bool v) AI {
#pragma unroll
    for (int p = 0; p < 4; ++p) if (g == DG[p] && (p < 2 ? k : v)) piece(p, t, prev);
  };
  auto ring_if = [&](int a, int b, int g, int t) AI {
#pragma unroll
    for (int p = 0; p < 4; ++p) if (g == DG[p]) {
      if (p < 2) dma16_if(a, b, ksrc, kbase + tl(t + 2 - kh) + 128 * p, wdst + prev + 1024 * p);
      else dma16_if(a, b, vsrc, vbase + tl(t) + 128 * (p & 1), (p & 1 ? vd1 : vd0) + prev); }
  };
#define TOPBAR(W) do { if (!kh) { W; } } while (0)
#define WPC (X4 ? 0x76543321000000ull : 0x77765432100000ull)
#define TC (X4 ? 16 : 22)
#define XN 5
#define XZ -1
#define XU ((bid * 37) & 255)
#define XF xu
  unsigned xu = XU; asm("" : "+s"(xu));
  const int ts = X4 ? 4 * max((int)(XF * XN >> 8) - XZ, 0) : 0;
  if (ts == 0) wrap = 0;
  auto barn = [&](int j, auto n) AI {
    const int e = ts;
    if (!X4) WAIT_BAR(n.value);
    else asm volatile("s_waitcnt vmcnt(%2+4)\n\ts_cmp_le_i32 %0, %1\n\ts_cbranch_scc1 .Lmha_w%=\n\ts_waitcnt vmcnt(%2)\n.Lmha_w%=:\n\ts_waitcnt lgkmcnt(0)\n\ts_barrier"
                      :: "s"(j), "s"(e), "i"(n.value) : "memory", "scc"); };
  auto bar = [&](int j) AI { barn(j, IC<5>{}); };
  if (X4 && !kh) { asm volatile("s_barrier" ::: "memory"); SBAR(); }
  step(IC<1>{}, SA, SB, [&] { barn(1, IC<X4 ? 8 : 4>{}); }, [&](int g) AI { if (X4) ring_if(0, ts, g, 3); else ring(g, 2, true, true); });
#define PC 180
  if (PI < 16 && wid == 0) {
    asm volatile("s_waitcnt vmcnt(4)" : "+v"(post));
    const u64 now = __builtin_amdgcn_s_memrealtime();
    const bool ok = now - post < 800;
    int d = ok ? (unsigned)now - (unsigned)post : 0;
    d += __builtin_amdgcn_update_dpp(0, d, 0x111, 15, 15, true); d += __builtin_amdgcn_update_dpp(0, d, 0x112, 15, 15, true);
    d += __builtin_amdgcn_update_dpp(0, d, 0x114, 15, 15, true); d += __builtin_amdgcn_update_dpp(0, d, 0x118, 15, 15, true);
    const int n = __builtin_popcount((unsigned)__builtin_amdgcn_ballot_w64(ok) & 0xffffu);
    const int m = __builtin_amdgcn_readlane((int)((float)d * __builtin_amdgcn_rcpf((float)n)), 15);
    if (n > 3) pdl += (unsigned)now - m + PC - tstart - PT0;
  }
  pace(); TOPBAR(BAR_ONLY());
  step(IC<1>{}, SB, SA, [&] { barn(2, IC<4>{}); }, [&](int g) AI { ring(g, 3 + (ts > 0), true, true); });
  if (!X4 && !kh) asm volatile("s_waitcnt vmcnt(4)" ::: "memory");
  const char* cod; unsigned cn_, cn; CODE_AT(".Lmha_last", cod, cn);
  const unsigned wb0 = wo_off(0), wb1 = wo_off(4);
#define FG 1.f
#define FS0 0x100e0c0a
#define FS1 0x08080906
#define FD 2
#define FMAX 117
#define FMIN 108
  u64 fb = 0; unsigned infl = 0, lead = ts > 0;
  unsigned mp = wid == 1 && xu >= 224 ? 0x1dddc : 0, ml = wid == 0 ? 0x44440 : 0; asm("" : "+s"(mp), "+s"(ml));
  auto probe = [&](int t, bool last) AI { if (mp >> t & 1) {
    unsigned c; asm volatile("s_getreg_b32 %0, hwreg(HW_REG_IB_STS, 0, 4)" : "=s"(c));
    infl += c;
    if (last) {
      const unsigned l = lane_id();
      const u64 v = __builtin_amdgcn_s_memrealtime() << 6 | infl; infl = 0;
      if (l < 8) asm volatile("global_store_dwordx2 %0, %1, %2 sc1\n\ts_nop 1" :: "v"(l * (4 * FST)), "v"(v), "s"(ws + WS_L + (t * 8 + (xu & 3)) * FST + (xu & 28) * 2) : "memory"); } } };
#pragma unroll 1
  for (int t = 2; t < 28; t += 2) {
    if (t == ts) { wrap = 0; lead = 0; }
    if (wid == 0) {
      if (0x111100 >> t & 1) {
        asm volatile("s_waitcnt vmcnt(9)" : "+v"(fb));
        const bool ok = (__builtin_amdgcn_s_memrealtime() << 6) - fb < (1000 << 6);
        int d = ok ? (unsigned)fb & 63 : 0;
        d += __builtin_amdgcn_update_dpp(0, d, 0x111, 15, 15, true); d += __builtin_amdgcn_update_dpp(0, d, 0x112, 15, 15, true);
        d += __builtin_amdgcn_update_dpp(0, d, 0x114, 15, 15, true); d += __builtin_amdgcn_update_dpp(0, d, 0x118, 15, 15, true);
        d += __builtin_amdgcn_update_dpp(0, d, 0x142, 10, 15, true);
        const int n = __builtin_popcount((unsigned)__builtin_amdgcn_ballot_w64(ok));
        const int m = __builtin_amdgcn_readlane((int)((float)(max(2 * d - n * (FS0 >> 2 * t - 16 & 255), 0) + FD * min(2 * d - n * (FS1 >> 2 * t - 16 & 255), 0)) * __builtin_amdgcn_rcpf((float)n * (2.f / FG))), 31);
        if (n > 15) per = min(max((int)per + m, FMIN), FMAX);
      }
      pace();
      if (0x44440 >> t & 1) { const unsigned l = lane_id();
        asm volatile("global_load_dwordx2 %0, %1, %2 sc1" : "+v"(fb) : "v"((l & 3) * FST + (l & 28) * 2), "s"(ws + WS_L + (t * 8 - 16 + xg * 4) * FST)); }
    }
    TOPBAR(bar(t));
    probe(t, !(t & 2));
    const int ta = t + lead;
    step(IC<1>{}, SA, SB, [&] { bar(t + 1); }, [&](int g) AI { if (X4) ring_if(t, ts, g, ta + 2); else ring(g, t + 2, true, true);
      if (g == DG[3] + 1) { const unsigned i = (unsigned)(WPC >> 2 * t); const bool tc = t == TC;
        const unsigned wb = (i & 4 ? wb1 : wb0) + (i & 2) * 65536 ^ (i & 1) * 128;
        dma16<false>(tc ? min(wid * 4096 + lane_id() * 64, cn) : wosrc, tc ? cod : wo + wb, wdst + P3 + (i & 6) * 8192 + (i & 1) * 1024); } });
    pace();
    if (ml >> t & 1) barn(t + 1, IC<6>{}); else TOPBAR(bar(t + 1));
    probe(t + 1, false);
    step(IC<1>{}, SB, SA, [&] { bar(t + 2); }, [&](int g) AI { ring(g, ta + 3, true, true); });
  }
  asm volatile(".Lmha_last:");
  if (PEND > 27) pace();
  TOPBAR(WAIT_BAR(5));
  step(IC<1>{}, SA, SB, [&] { WAIT_BAR(8); }, [&](int g) AI { ring(g, 30, kh, true); if (g >= 8 && g < 16 && !(g & 1)) ld_w((g - 8) >> 1); });
  if (PEND > 28) pace();
  TOPBAR(WAIT_BAR(6));
  step(IC<1>{}, SB, SA, [&] { WAIT_BAR(10); }, [&](int g) AI { ring(g + DG[2], 31, false, true); if (g >= 8 && g < 16 && !(g & 1)) ld_w(4 + ((g - 8) >> 1)); });
  if (PEND > 29) pace();
  TOPBAR(WAIT_BAR(10));
  gu64* const fl1 = (gu64*)(ws + WS_F1) + (b * 4 + og) * 128, * const fl2 = (gu64*)(ws + WS_F2) + (b * 8 + oj) * 64, * const ep = (gu64*)(ws + WS_E) + b * 16;
  const unsigned ln = lane_id();
  const u64 ep0 = __hip_atomic_load(wid || ln < 16 ? ep : ln < 32 ? fl1 + (ln & 7) * 16 : fl2 + (ln & 3) * 16, RLX_AGENT);
  step(IC<0>{}, SA, SB, [&] { WAIT_BAR(5); }, [&](int g) AI { if (g >= 16 && !(g & 1)) ld_w(8 + ((g - 16) >> 1)); });
  if (PEND > 30) pace();
  TOPBAR(WAIT_BAR(13));

  const __amdgpu_buffer_rsrc_t wsr = __builtin_amdgcn_make_buffer_rsrc(ws, 0, WS_END, 0x00020000);
  const unsigned fo = wid * 4096 + ln * 16, obase = WS_O + (b * 4 + og) * (8 * 32768);
  u32x4 of[8][4];
  u64 fv;
  const unsigned tch = __builtin_amdgcn_raw_buffer_load_b32(wor, ((ln >> 2) & 7) * 2048 + (ln & 3) * 64, wo_off(24 + 2 * (jj & 3)) & ~255u, 0);
  SBAR();
  {
    const unsigned EL = X4 ? slot : P1, EA = X4 ? prev : P0, EB = P1 + P2 - EL - EA;
    LAS f32x4* const ex = (LAS f32x4*)(lds + (kh ? EB : EA) + qb * 8192 + ln * 16);
    const LAS f32x4* const pex = (const LAS f32x4*)(lds + (kh ? EA : EB) + qb * 8192 + ln * 16);
    LAS f32x4* const st = (LAS f32x4*)(lds + EL + wid * 1024 + ln * 16);
    const LAS f32x4* const pst = (const LAS f32x4*)(lds + EL + (wid ^ 4) * 1024 + ln * 16);
    constexpr int XL = LSM ? 21 : XG[15] + 2;
    float mx, f1[2], f2[2];
    f32x4 pv[8];
    f32x4 ps;
    auto factors = [&](int j) {
      const float mn = fmaxf(mref[j], ps[j]), a1 = __builtin_amdgcn_exp2f(mref[j] - mn), a2 = __builtin_amdgcn_exp2f(ps[j] - mn);
      const float inv = __builtin_amdgcn_rcpf(lsum[j] * a1 + ps[2 + j] * a2);
      f1[j] = a1 * inv; f2[j] = a2 * inv; PIN(f1[j]); PIN(f2[j]);
    };
    gaps(IC<0>{}, IC<XL>{}, IC<0>{}, IC<0>{}, SB, SA, mx, [](int) {});
    if (LSM) { lsum[0] = ls4[0][0]; lsum[1] = ls4[1][0]; }
    if (LSM < 2) { lsum[0] = rowsum4(lsum[0]); lsum[1] = rowsum4(lsum[1]); }
    *st = (f32x4){mref[0], mref[1], lsum[0], lsum[1]};
    SBAR();
    if (kh) BAR_ONLY();
    gaps(IC<XL>{}, IC<32>{}, IC<0>{}, IC<0>{}, SB, SA, mx, [&](int g) AI { const int p = g - 24;
      if (g == XL) ps = *pst;
      if (g == XL + 4 || g == XL + 5) factors(g - XL - 4);
      if (p >= 0) { ex[(2 * (p & 3) + (p >> 2)) * 64] = o[4 + (p & 3)][p >> 2]; pv[p] = pex[p * 64]; } });
    if (!kh) { BAR_ONLY(); ps = *pst; factors(0); factors(1); }
    BAR_ONLY();
    if (!kh) {
#pragma unroll
      for (int i = 0; i < 8; ++i) pv[i] = pex[i * 64];
    }
#pragma unroll
    for (int i = 0; i < 8; ++i) pv[i] = pv[i] * f2[i & 1] + o[i >> 1][i & 1] * f1[i & 1];
#pragma unroll
    for (int i = 0; i < 4; ++i) {
      const f32x4 u = pv[4 * (i >> 1) + (i & 1)], v = pv[4 * (i >> 1) + 2 + (i & 1)];
      of[0][i] = (u32x4){cvtpk(u[0], u[1]), cvtpk(u[2], u[3]), cvtpk(v[0], v[1]), cvtpk(v[2], v[3])};
      st16(of[0][i], wsr, fo + i * 1024, obase + pos * 32768);
    }
    BAR_ONLY();
  }
#define NL 3
#define PD 6
#define PK0 4
#define XBAR 4
  constexpr int F0 = 16 * (8 - NL);
  constexpr auto fs = [](int F) { return F < F0 ? F >> 4 : 8 - NL + ((F - F0) >> 2) % NL; };
  constexpr auto fj = [](int F) { return F < F0 ? (F >> 3) & 1 : (F >> 1) & 1; };
  constexpr auto fn = [](int F) { return F < F0 ? F & 7 : 2 * ((F - F0) / (4 * NL)) + (F & 1); };
  f32x4 acc[8][2];
  bf16x8 af[8];
  const unsigned l4 = ln >> 4;
  const unsigned wosrc2 = ((ln >> 2) & 7) * 2048 + ((ln >> 5) ^ (~og & 1)) * 64 + ((ln & 3) ^ ((ln >> 3) & 2)) * 16;
  const lds_char* wa_ = lds + 16384 * kh + 2048 * (l4 >> 1) + 256 * (l4 & 1) + 64 * ((ln & 15) >> 2) + 8 * (ln & 3);
  const LdsBase wa0(wa_ + 32 * ((l4 ^ kh) & 1)), wa1(wa_ + 32 * ((l4 ^ kh ^ 1) & 1));
  const LdsBase ww(lds + wid * 2048 + ln * 16);
  constexpr unsigned WP[8] = {P3, P4, P0, P1, P2, P3, P4, P0};
  auto rd_h = [&](int F, int hi) { return lds_tr(((F & 1) ? wa1 : wa0).at(WP[fs(F)] + 8192 * fj(F) + 512 * (fn(F) >> 1) + 4096 * hi)); };
  auto wr_w = [&](int i) { *(LAS u32x4*)ww.at(WP[2 + (i >> 2)] + ((i >> 1) & 1) * 16384 + (i & 1) * 1024) = wl[i]; };
  auto frag = [&](int F) AI {
    s16x4 lo_;
#pragma unroll
    for (int qj = 0; qj < 2; ++qj) {
      const int q_ = qj;
      acc[fn(F)][q_] = MFMA16(af[F & 7], asb(of[fs(F)][2 * fj(F) + q_]), F < 8 ? (f32x4)(0.f) : acc[fn(F)][q_]);
      SBAR();
      if (F + PD < 128) { if (qj == 0) lo_ = rd_h(F + PD, 0); else af[(F + PD) & 7] = cat8(lo_, rd_h(F + PD, 1)); }
      SBAR();
    }
  };
  u64 tag = (((u64)rfl((unsigned)(ep0 >> 32)) << 32) | rfl((unsigned)ep0)) + 1;
  asm volatile("" : "+s"(tag));
  int rdy = 0;
  auto look = [&](u64 v) { rdy = __builtin_ctz(~((unsigned)__builtin_amdgcn_ballot_w64(v == tag) & 255u | 1u << pos)); };
#define FW 1
  gu64* const pl1 = fl1 + (FW && (ln & 7) == pos ? pos ^ 1 : ln & 7) * 16;
  auto peek = [&] { if (wid == 0) fv = __hip_atomic_load(pl1, RLX_AGENT); };
  auto need = [&](int s) AI {
    const int r = rank_of(s);
    if (wid == 0 && rdy <= r) {
      look(fv);
#pragma unroll 1
      for (unsigned spins = 0; rdy <= r && spins < SPIN_CAP; ++spins) look(__hip_atomic_load(pl1, RLX_AGENT));
    }
  };
  auto ld_o = [&](int i) { of[i >> 2][i & 3] = __builtin_amdgcn_raw_buffer_load_b128(wsr, fo + (i & 3) * 1024, obase + rank_of(i >> 2) * 32768, 16); };
  SBAR();
#pragma unroll
  for (int i = 0; i < 12; ++i) { if (i == PK0) { peek(); SBAR(); } wr_w(i); SBAR(); }
#pragma unroll
  for (int F = 0; F < PD; ++F) af[F] = cat8(rd_h(F, 0), rd_h(F, 1));
  SBAR();
  LAS f32x4* const ex = (LAS f32x4*)(lds + P1 + wid * 4096 + ln * 16);
  const LAS f32x4* const pex = (const LAS f32x4*)(lds + P1 + (wid ^ 4) * 4096 + ln * 16);
  const unsigned lin = wid * 1024 + ln * 16;
  const unsigned pbase = WS_P + (b * 8 + oj) * 131072;
  f32x4 pv[2];
  auto send = [&](int t) {
    const f32x4 r0 = acc[2 * t][0] + pv[0], r1 = acc[2 * t][1] + pv[1];
    st16((u32x4){cvtpk(r0[0], r0[1]), cvtpk(r0[2], r0[3]), cvtpk(r1[0], r1[1]), cvtpk(r1[2], r1[3])}, wsr, lin, pbase + ((t ^ og ^ 3) * 4 + og) * 8192);
  };
#define OPT {40, 56, 16, 24, 32, 48, 64, 72}
#define OPD 2
  constexpr auto opf = [](int n) { constexpr int T[8] = OPT; return T[n >> 2] + OPD * (n & 3); };
  constexpr auto younger_ops = [=](int f0, int f1) { int c = 0; for (int n = 0; n < 32; ++n) c += opf(n) > f0 && opf(n) <= f1; return c; };
#define BRT {0, 16, 32, 48, 64, 80}
#define WF5 16
#define VW6 64
#define VW7 80
#define PKB 13
  constexpr auto nextbar = [](int f) { constexpr int T[] = BRT; for (int x : T) if (x > f) return x; return 128; };
  constexpr auto lastbar = [](int f) { constexpr int T[] = BRT; int r = 0; for (int x : T) if (x <= f) r = x; return r; };
  constexpr auto needs = [=](int f) { int s = f == 0; for (int g = 2; g < 8; ++g) if (opf(4 * g) >= f && opf(4 * g) < nextbar(f)) s = g; return s; };
  sfor<0, 128>([&](auto fc) AI {
    constexpr int F = fc.value, t = F < F0 ? -1 : (F - F0) / (4 * NL) - 1, ph = F < F0 ? -1 : (F - F0) % (4 * NL);
    if constexpr (F == 0) {
      WAIT_BAR(0);
      use(tch);
      if (wid == 0) look(fv);
      if (wid == FW && ln == 0) __hip_atomic_store(fl1 + pos * 16, tag, RLX_AGENT);
    }
    if constexpr (lastbar(F) == F) { if constexpr (needs(F) > 0) need(needs(F)); }
    if (lastbar(F) == F) asm volatile("s_barrier" ::: "memory");
    {
      constexpr auto pr = [=](int f) { const int x0 = F0 + 4 * NL + XBAR, l = lastbar(f), n = nextbar(f) < 128 ? nextbar(f) : x0;
        return f < n ? 3 - 4 * (f - l) / (n - l) : 3 - ((f - x0) % (4 * NL)) / NL; };
      if constexpr (F == 0 || pr(F) != pr(F - 1)) __builtin_amdgcn_s_setprio(pr(F));
    }
    if constexpr (F == 0) {
#pragma unroll
      for (int i = 4; i < 8; ++i) ld_o(i);
      SBAR();
    }
    if constexpr (F == WF5) {
#pragma unroll
      for (int i = 12; i < 16; ++i) wr_w(i);
      SBAR();
    }
    if (t >= 0 && ph == XBAR) {
      constexpr int younger = 2 * ((F + PD < 128 ? F + PD : 128) - (F - XBAR + 2 + PD < 128 ? F - XBAR + 2 + PD : 128));
      asm volatile("s_waitcnt lgkmcnt(%0)\n\ts_barrier" :: "i"(younger) : "memory");
      pv[0] = pex[(t & 1) * 128]; pv[1] = pex[(t & 1) * 128 + 64];
    }
    frag(F);
    if constexpr (F + PKB == nextbar(F) && F + PKB < 128) { if constexpr (needs(F + PKB) > 0) peek(); }
    sfor<0, 32>([&](auto nc) AI { constexpr int g = nc.value >> 2, i = 4 * (g & 7) + (nc.value & 3);
      if constexpr (opf(nc.value) == fc.value) {
        if constexpr (g >= 2) ld_o(i);
        else dma16<false>(wosrc2, wo + wo_off(24 + i), wdst + WP[6 + g] + ((i >> 1) & 1) * 16384 + (i & 1) * 1024); } });
    if constexpr (F == VW6 - 1) asm volatile("s_waitcnt vmcnt(%0)" :: "i"(younger_ops(opf(3), F)) : "memory");
    if constexpr (F == VW7 - 1) asm volatile("s_waitcnt vmcnt(%0)" :: "i"(younger_ops(opf(7), F)) : "memory");
    if constexpr (t >= 0) {
      if (ph == 1) { ex[(t & 1) * 128] = acc[2 * t + 1][0]; ex[(t & 1) * 128 + 64] = acc[2 * t + 1][1]; }
      if (ph == XBAR + 3) send(t);
    }
    SBAR();
  });
  {
    ex[128] = acc[7][0]; ex[192] = acc[7][1];
    WAIT_BAR(0);
#define FW2 FW
    if (wid == FW2 && ln == 0) __hip_atomic_store(fl2 + og * 16, tag, RLX_AGENT);
    if (wid == 0) {
#pragma unroll 1
      for (unsigned spins = 0; spins < SPIN_CAP; ++spins)
        if (((unsigned)__builtin_amdgcn_ballot_w64(__hip_atomic_load(fl2 + (FW2 && (ln & 3) == og ? og ^ 1 : ln & 3) * 16, RLX_AGENT) == tag) & 15u | 1u << og) == 15u) break;
      if (h == 0 && ln == 0) __hip_atomic_store(ep, tag, RLX_AGENT);
    }
    f32x4 v[2] = {acc[6][0] + pex[128], acc[6][1] + pex[192]};
    BAR_ONLY();
    u32x4 pl[3];
#pragma unroll
    for (int i = 0; i < 3; ++i) pl[i] = __builtin_amdgcn_raw_buffer_load_b128(wsr, lin, pbase + (og * 4 + ((og + 1 + i) & 3)) * 8192, 16);
#pragma unroll
    for (int i = 0; i < 3; ++i)
#pragma unroll
      for (int e = 0; e < 2; ++e)
        v[e] += (f32x4){__uint_as_float(pl[i][2 * e] << 16), __uint_as_float(pl[i][2 * e] & 0xffff0000u),
                        __uint_as_float(pl[i][2 * e + 1] << 16), __uint_as_float(pl[i][2 * e + 1] & 0xffff0000u)};
    asm volatile(".Lmha_end:");
    auto s0 = __builtin_amdgcn_permlane16_swap(cvtpk(v[0][0], v[0][1]), cvtpk(v[1][0], v[1][1]), false, false);
    auto s1 = __builtin_amdgcn_permlane16_swap(cvtpk(v[0][2], v[0][3]), cvtpk(v[1][2], v[1][3]), false, false);
#define OAUX 0
    __builtin_amdgcn_raw_buffer_store_b128((u32x4){s0[0], s1[0], s0[1], s1[1]}, __builtin_amdgcn_make_buffer_rsrc(out, 0, 8 * SQ * DM * 2, 0x00020000),
      (ln & 31) * (DM * 2) + 16 * (ln >> 5), ((b * SQ + 32 * qb) * DM + 128 * oj + 32 * og + 16 * kh) * 2, OAUX);
  }
}

extern "C" void kernel_launch(void* const* d_in, const int* in_sizes, int n_in,
                              void* d_out, int out_size, void* d_ws, size_t ws_size,
                              hipStream_t stream) {
  (void)hipFuncSetAttribute((const void*)mha_kernel, hipFuncAttributeMaxDynamicSharedMemorySize, LDS1);
  mha_kernel<<<256, 512, LDS1, stream>>>((const char*)d_in[0], (const char*)d_in[1], (const char*)d_in[2], (const char*)d_in[3],
                                         (const char*)d_in[4], (char*)d_ws, (char*)d_out);
}
```
